```python
import torch

def _softmax(x, axis=-1):
    return torch.softmax(x, dim=axis)
def _gelu(x):
    return torch.nn.functional.gelu(x, approximate="tanh")
def _silu(x):
    return torch.nn.functional.silu(x)
def _sigmoid(x):
    return torch.sigmoid(x)
def _layernorm(x, axis=-1, eps=1e-5):
    m = x.mean(dim=axis, keepdim=True)
    v = x.var(dim=axis, keepdim=True, unbiased=False)
    return (x - m) / torch.sqrt(v + eps)
def _scatter_add(tgt, idx, src, axis=0):
    return tgt.clone().index_add_(axis, idx, src)
def _segment_sum(src, seg, n):
    out = torch.zeros((n,) + src.shape[1:], dtype=src.dtype)
    return out.index_add_(0, seg, src)
def _pad(x, pw):
    flat = tuple(v for pair in reversed(pw) for v in pair)
    return torch.nn.functional.pad(x, flat)

kernel_name = 'concat_heads__gqa_block_cached__gqa_block_cached__near_ridge'

def setup_inputs():

    _w = (lambda a: (lambda t: t.to(torch.bfloat16) if t.is_floating_point() else t)(torch.as_tensor(a)))
    def _A_setup():
        B, Sq, D = 16, 48, 512
        Hq, Hkv, Sk, Dh = 32, 8, 512, 128
        assert Hq % Hkv == 0, (Hq, Hkv)
        return {
            "x": _w((lambda *s: torch.randn(*s, dtype=torch.bfloat16) * 0.8)(B, Sq, D)),
            "k_cache": _w((lambda *s: torch.randn(*s, dtype=torch.bfloat16) * 0.8)(B, Hkv, Sk, Dh)),
            "v_cache": _w((lambda *s: torch.randn(*s, dtype=torch.bfloat16) * 0.05)(B, Hkv, Sk, Dh)),
            "wq": _w((lambda *s: torch.randn(*s, dtype=torch.bfloat16) * 0.8)(D, Hq * Dh) * (D ** -0.5)),
            "wo": _w((lambda *s: torch.randn(*s, dtype=torch.bfloat16) * 0.05)(Hq * Dh, D) * ((Hq * Dh) ** -0.5)),
        }
    _A_in = _A_setup()

    def _B_setup():
        B, Sq, D = 16, 48, 512
        Hq, Hkv, Sk, Dh = 32, 8, 512, 128
        assert Hq % Hkv == 0, (Hq, Hkv)
        return {
            "x": _w((lambda *s: torch.randn(*s, dtype=torch.bfloat16) * 0.8)(B, Sq, D)),
            "k_cache": _w((lambda *s: torch.randn(*s, dtype=torch.bfloat16) * 0.8)(B, Hkv, Sk, Dh)),
            "v_cache": _w((lambda *s: torch.randn(*s, dtype=torch.bfloat16) * 0.05)(B, Hkv, Sk, Dh)),
            "wq": _w((lambda *s: torch.randn(*s, dtype=torch.bfloat16) * 0.8)(D, Hq * Dh) * (D ** -0.5)),
            "wo": _w((lambda *s: torch.randn(*s, dtype=torch.bfloat16) * 0.05)(Hq * Dh, D) * ((Hq * Dh) ** -0.5)),
        }
    _B_in = _B_setup()

    return {
        "A_x": _A_in["x"],
        "A_k_cache": _A_in["k_cache"],
        "A_v_cache": _A_in["v_cache"],
        "A_wq": _A_in["wq"],
        "A_wo": _A_in["wo"],
        "B_x": _B_in["x"],
        "B_k_cache": _B_in["k_cache"],
        "B_v_cache": _B_in["v_cache"],
        "B_wq": _B_in["wq"],
        "B_wo": _B_in["wo"],
    }

def reference(A_x, A_k_cache, A_v_cache, A_wq, A_wo, B_x, B_k_cache, B_v_cache, B_wq, B_wo):
    def _A_ref(x, k_cache, v_cache, wq, wo):
        B, Sq, D = x.shape
        Hq, Dh = 32, 128
        Hkv = k_cache.shape[1]
        rep = Hq // Hkv
        q = (lambda x, *d: x.permute(*d))((x @ wq).reshape(B, Sq, Hq, Dh), 0, 2, 1, 3)
        k = (lambda x, n, axis: torch.repeat_interleave(x, n, dim=axis))(k_cache, rep, axis=1)
        v = (lambda x, n, axis: torch.repeat_interleave(x, n, dim=axis))(v_cache, rep, axis=1)
        scores = torch.einsum("bhqd,bhkd->bhqk", q, k) * (Dh ** -0.5)
        p = _softmax(scores, axis=-1)
        o = torch.einsum("bhqk,bhkd->bhqd", p, v)
        return (lambda x, *d: x.permute(*d))(o, 0, 2, 1, 3).reshape(B, Sq, Hq * Dh) @ wo

    def _B_ref(x, k_cache, v_cache, wq, wo):
        B, Sq, D = x.shape
        Hq, Dh = 32, 128
        Hkv = k_cache.shape[1]
        rep = Hq // Hkv
        q = (lambda x, *d: x.permute(*d))((x @ wq).reshape(B, Sq, Hq, Dh), 0, 2, 1, 3)
        k = (lambda x, n, axis: torch.repeat_interleave(x, n, dim=axis))(k_cache, rep, axis=1)
        v = (lambda x, n, axis: torch.repeat_interleave(x, n, dim=axis))(v_cache, rep, axis=1)
        scores = torch.einsum("bhqd,bhkd->bhqk", q, k) * (Dh ** -0.5)
        p = _softmax(scores, axis=-1)
        o = torch.einsum("bhqk,bhkd->bhqd", p, v)
        return (lambda x, *d: x.permute(*d))(o, 0, 2, 1, 3).reshape(B, Sq, Hq * Dh) @ wo

    a = _A_ref(A_x, A_k_cache, A_v_cache, A_wq, A_wo)
    b = _B_ref(B_x, B_k_cache, B_v_cache, B_wq, B_wo)
    return torch.concatenate([a, b], axis=-1)
```

```cpp
#include <hip/hip_runtime.h>
#include <hip/hip_bf16.h>

typedef __bf16   bf16x8 __attribute__((ext_vector_type(8)));
typedef __bf16   bf16x2 __attribute__((ext_vector_type(2)));
typedef short    s16x4  __attribute__((ext_vector_type(4)));
typedef float    f32x4  __attribute__((ext_vector_type(4)));
typedef float    f32x2  __attribute__((ext_vector_type(2)));
typedef unsigned u32x4  __attribute__((ext_vector_type(4)));
typedef unsigned u32x2  __attribute__((ext_vector_type(2)));
#define LAS __attribute__((address_space(3)))
#define DI __device__ __forceinline__

namespace {
constexpr int NB = 16, SQ = 48, DM = 512, HQ = 32, HKV = 8, SK = 512, DH = 128;
constexpr int XO_BYTES = SQ * DM * 2;
constexpr int SLOT = 16384, NSLOT = 7;
constexpr int RING = XO_BYTES;
constexpr int LDS_BYTES = XO_BYTES + NSLOT * SLOT;
constexpr float CSC = 0.08838834764831845f * 1.4426950408889634f;
constexpr float THR = 8.0f;
constexpr size_t SLAB_F = (size_t)SQ * DM;

DI unsigned cvtpk(float lo, float hi) {
  f32x2 v = {lo, hi}; bf16x2 b = __builtin_convertvector(v, bf16x2); return __builtin_bit_cast(unsigned, b);
}
DI f32x4 mfma(u32x4 a, u32x4 b, f32x4 c) {
  return __builtin_amdgcn_mfma_f32_16x16x32_bf16(__builtin_bit_cast(bf16x8, a), __builtin_bit_cast(bf16x8, b), c, 0, 0, 0);
}
DI float max3f(float a, float b, float c) { float r; asm("v_max3_f32 %0, %1, %2, %3" : "=v"(r) : "v"(a), "v"(b), "v"(c)); return r; }
DI float max2f(float a, float b) { float r; asm("v_max_f32_e32 %0, %1, %2" : "=v"(r) : "v"(a), "v"(b)); return r; }
DI void bar() { __builtin_amdgcn_s_barrier(); }
#define WAIT_L0() asm volatile("s_waitcnt lgkmcnt(0)" ::: "memory")
#define WAIT_V(n) asm volatile("s_waitcnt vmcnt(" #n ")" ::: "memory")
#define SB0() __builtin_amdgcn_sched_barrier(0)

DI __amdgpu_buffer_rsrc_t mk_rsrc(const void* p, unsigned bytes) {
  return __builtin_amdgcn_make_buffer_rsrc((void*)p, (short)0, (int)bytes, 0x00020000);
}
DI void dma16(__amdgpu_buffer_rsrc_t rs, char* lds_dst, unsigned voff, unsigned soff) {
  __builtin_amdgcn_raw_ptr_buffer_load_lds(rs, (LAS void*)lds_dst, 16, (int)voff, (int)soff, 0, 0);
}

template <unsigned WSTRIDE>
DI void load_w(u32x4 (&w)[8], __amdgpu_buffer_rsrc_t rs, unsigned voff, int ks) {
#pragma unroll
  for (int j = 0; j < 8; ++j)
    w[j] = __builtin_amdgcn_raw_buffer_load_b128(rs, (int)voff, (int)((unsigned)(32 * ks + j) * WSTRIDE), 0);
}
DI void perm_d(u32x4& f0, u32x4& f1, const u32x4 (&w)[8], int d) {
#pragma unroll
  for (int p = 0; p < 4; ++p) {
    f0[p] = __builtin_amdgcn_perm(w[2 * p + 1][d], w[2 * p][d], 0x05040100u);
    f1[p] = __builtin_amdgcn_perm(w[2 * p + 1][d], w[2 * p][d], 0x07060302u);
  }
}
DI void mma_d(f32x4 (&acc)[3][8], const u32x4& f0, const u32x4& f1, const u32x4 (&xb)[3], int d) {
#pragma unroll
  for (int qt = 0; qt < 3; ++qt) {
    acc[qt][2 * d]     = mfma(f0, xb[qt], acc[qt][2 * d]);
    acc[qt][2 * d + 1] = mfma(f1, xb[qt], acc[qt][2 * d + 1]);
  }
}
DI void read_x(u32x4 (&xb)[3], const char* lds, const unsigned (&xa)[4], int ks) {
#pragma unroll
  for (int qt = 0; qt < 3; ++qt) xb[qt] = *(const u32x4*)(lds + xa[ks & 3] + (ks >> 2) * 256 + qt * 16384);
}
template <unsigned WSTRIDE, int KS>
DI void gemm_step(f32x4 (&acc)[3][8], u32x4 (&w)[8], u32x4 (&fa)[2], u32x4 (&fb)[2], u32x4 (&xc)[3], u32x4 (&xn)[3],
                  __amdgpu_buffer_rsrc_t rs, unsigned voff, const char* lds, const unsigned (&xa)[4]) {
  perm_d(fa[0], fa[1], w, 0);
  if (KS > 0) mma_d(acc, fb[0], fb[1], xn, 3);
  SB0();
  if (KS + 1 < 16) read_x(xn, lds, xa, KS + 1);
  perm_d(fb[0], fb[1], w, 1);
  mma_d(acc, fa[0], fa[1], xc, 0);
  SB0();
  perm_d(fa[0], fa[1], w, 2);
  mma_d(acc, fb[0], fb[1], xc, 1);
  SB0();
  perm_d(fb[0], fb[1], w, 3);
  mma_d(acc, fa[0], fa[1], xc, 2);
  SB0();
  if (KS + 2 < 16) load_w<WSTRIDE>(w, rs, voff, KS + 2);
  SB0();
  if (KS == 15) mma_d(acc, fb[0], fb[1], xc, 3);
}
template <unsigned WSTRIDE>
DI void gemm_main(f32x4 (&acc)[3][8], u32x4 (&w0)[8], u32x4 (&w1)[8], __amdgpu_buffer_rsrc_t rs, unsigned voff, const char* lds, const unsigned (&xa)[4]) {
  u32x4 fa[2], fb[2], x0[3], x1[3];
  read_x(x0, lds, xa, 0);
#define GS2(k) gemm_step<WSTRIDE, k>(acc, w0, fa, fb, x0, x1, rs, voff, lds, xa); gemm_step<WSTRIDE, k + 1>(acc, w1, fa, fb, x1, x0, rs, voff, lds, xa);
  GS2(0) GS2(2) GS2(4) GS2(6) GS2(8) GS2(10) GS2(12) GS2(14)
#undef GS2
}
}

__global__ __launch_bounds__(512, 2) void gqa_fused_kernel(
    const unsigned short* __restrict__ xA, const unsigned short* __restrict__ kA, const unsigned short* __restrict__ vA,
    const unsigned short* __restrict__ wqA, const unsigned short* __restrict__ woA,
    const unsigned short* __restrict__ xB, const unsigned short* __restrict__ kB, const unsigned short* __restrict__ vB,
    const unsigned short* __restrict__ wqB, const unsigned short* __restrict__ woB,
    float* __restrict__ slab) {
  extern __shared__ __attribute__((aligned(16))) char lds[];
  const int tid = threadIdx.x, lane = tid & 63;
  const int wid = __builtin_amdgcn_readfirstlane(tid >> 6);
  const int li = lane & 15, g = lane >> 4;
  const int blk = blockIdx.x, kvh = blk & 7, rest = blk >> 3, br = rest >> 4, b = rest & 15;
  const unsigned short* x  = (br ? xB : xA) + (size_t)b * SQ * DM;
  const unsigned short* kp = (br ? kB : kA) + ((size_t)b * HKV + kvh) * SK * DH;
  const unsigned short* vp = (br ? vB : vA) + ((size_t)b * HKV + kvh) * SK * DH;
  const unsigned short* wq = (br ? wqB : wqA) + (size_t)kvh * 512;
  const unsigned short* wo = (br ? woB : woA) + (size_t)kvh * 512 * DM;
  const __amdgpu_buffer_rsrc_t rsK = mk_rsrc(kp, SK * DH * 2), rsV = mk_rsrc(vp, SK * DH * 2);

  unsigned xa[4];
#pragma unroll
  for (int bq = 0; bq < 4; ++bq) xa[bq] = (unsigned)(li * 1024 + 16 * ((4 * bq + g) ^ li));

  const int h = wid & 3, kh = wid >> 2;
  u32x4 qf[3][4];

  auto dma_tile = [&](int u, int p0, int np) {
    char* dst = lds + RING + (u % NSLOT) * SLOT;
    const int t = u >> 1;
#pragma unroll
    for (int i = 0; i < np; ++i) {
      const int p = p0 + i;
      const int row = 4 * p + g;
      if (u & 1) dma16(rsV, dst + p * 1024, (unsigned)(g * 256 + 16 * (li ^ (2 * (row & 7)))), (unsigned)((t * 64 + 4 * p) * 256));
      else       dma16(rsK, dst + p * 1024, (unsigned)(g * 256 + 16 * (li ^ (row & 15))), (unsigned)((t * 64 + 4 * p) * 256));
    }
  };

  if (wid >= 4) {
    const __amdgpu_buffer_rsrc_t rsX = mk_rsrc(x, SQ * DM * 2);
    const int w4 = wid - 4;
#pragma unroll
    for (int i = 0; i < 12; ++i) {
      const int row = w4 * 12 + i;
      dma16(rsX, lds + row * 1024, (unsigned)(16 * (lane ^ (row & 15))), (unsigned)(row * 1024));
    }
#pragma unroll
    for (int u = 0; u < NSLOT; ++u) dma_tile(u, w4 * 4, 4);
    WAIT_V(28);
    bar();
    bar();
  } else {
    const __amdgpu_buffer_rsrc_t rsW = mk_rsrc(wq, (unsigned)(DM * HQ * DH * 2 - kvh * 1024));
    const int cg = 4 * (li & 3) + (li >> 2);
    const unsigned voff = (unsigned)(8 * g * 8192 + wid * 256 + 16 * cg);
    u32x4 w0[8], w1[8];
    load_w<8192>(w0, rsW, voff, 0);
    load_w<8192>(w1, rsW, voff, 1);
    f32x4 acc[3][8];
#pragma unroll
    for (int qt = 0; qt < 3; ++qt)
#pragma unroll
      for (int c = 0; c < 8; ++c) acc[qt][c] = f32x4{0.f, 0.f, 0.f, 0.f};
    bar();
    gemm_main<8192>(acc, w0, w1, rsW, voff, lds, xa);
#pragma unroll
    for (int qt = 0; qt < 3; ++qt)
#pragma unroll
      for (int s = 0; s < 4; ++s) {
        u32x4 f;
#pragma unroll
        for (int p = 0; p < 4; ++p) f[p] = cvtpk(acc[qt][2 * p][s], acc[qt][2 * p + 1][s]);
        qf[qt][s] = f;
      }
    WAIT_L0();
    bar();
#pragma unroll
    for (int qt = 0; qt < 3; ++qt)
#pragma unroll
      for (int s = 0; s < 4; ++s) *(u32x4*)(lds + wid * 12288 + (qt * 4 + s) * 1024 + lane * 16) = qf[qt][s];
    WAIT_L0();
  }

  unsigned ka[4];
#pragma unroll
  for (int s = 0; s < 4; ++s) ka[s] = (unsigned)(RING + (kh * 32 + li) * 256 + 16 * ((4 * s + g) ^ li));
  const int vr = 4 * g + (li >> 2);
  const unsigned va = (unsigned)(RING + (kh * 32 + vr) * 256 + 32 * (vr & 7) + 8 * (li & 3));

  f32x4 oacc[8][3];
#pragma unroll
  for (int dt = 0; dt < 8; ++dt)
#pragma unroll
    for (int qt = 0; qt < 3; ++qt) oacc[dt][qt] = f32x4{0.f, 0.f, 0.f, 0.f};
  float mS[3], lsum[3], mlim[3];
#pragma unroll
  for (int qt = 0; qt < 3; ++qt) { mS[qt] = -__builtin_inff(); lsum[qt] = 0.f; mlim[qt] = -__builtin_inff(); }

#pragma unroll
  for (int t = 0; t < 8; ++t) {
    if (t == 0) WAIT_V(20); else if (t == 1) WAIT_V(12); else if (t == 2) WAIT_V(8); else if (t <= 5) WAIT_V(6); else if (t == 6) WAIT_V(4); else WAIT_V(0);
    WAIT_L0();
    bar();
    if (t == 0 && wid >= 4) {
#pragma unroll
      for (int qt = 0; qt < 3; ++qt)
#pragma unroll
        for (int s = 0; s < 4; ++s) qf[qt][s] = *(const u32x4*)(lds + h * 12288 + (qt * 4 + s) * 1024 + lane * 16);
    }
    if (t >= 1) {
      if (2 * t + 5 < 16) dma_tile(2 * t + 5, wid * 2, 2);
      if (2 * t + 6 < 16) dma_tile(2 * t + 6, wid * 2, 2);
    }
    const unsigned ks_off = (unsigned)(((2 * t) % NSLOT) * SLOT), vs_off = (unsigned)(((2 * t + 1) % NSLOT) * SLOT);
    f32x4 S[2][3];
#pragma unroll
    for (int kt = 0; kt < 2; ++kt) {
#pragma unroll
      for (int qt = 0; qt < 3; ++qt) S[kt][qt] = f32x4{0.f, 0.f, 0.f, 0.f};
#pragma unroll
      for (int s = 0; s < 4; ++s) {
        const u32x4 kf = *(const u32x4*)(lds + ka[s] + ks_off + kt * 4096);
#pragma unroll
        for (int qt = 0; qt < 3; ++qt) S[kt][qt] = mfma(kf, qf[qt][s], S[kt][qt]);
      }
    }
    float mx[3];
    bool need = false;
#pragma unroll
    for (int qt = 0; qt < 3; ++qt) {
      float a = max3f(S[0][qt][0], S[0][qt][1], S[0][qt][2]);
      a = max3f(a, S[0][qt][3], S[1][qt][0]);
      a = max3f(a, S[1][qt][1], S[1][qt][2]);
      mx[qt] = max2f(a, S[1][qt][3]);
      need |= mx[qt] > mlim[qt];
    }
    if (__any(need)) {
#pragma unroll
      for (int qt = 0; qt < 3; ++qt) {
        float m = mx[qt];
        m = fmaxf(m, __shfl_xor(m, 16));
        m = fmaxf(m, __shfl_xor(m, 32));
        const float mn = fmaxf(mS[qt], m * CSC);
        const float al = __builtin_amdgcn_exp2f(mS[qt] - mn);
        mS[qt] = mn; mlim[qt] = (mn + THR) * (1.0f / CSC);
        lsum[qt] *= al;
#pragma unroll
        for (int dt = 0; dt < 8; ++dt) oacc[dt][qt] *= al;
      }
    }
    u32x4 pf[3];
#pragma unroll
    for (int qt = 0; qt < 3; ++qt) {
      float p[8];
#pragma unroll
      for (int j = 0; j < 8; ++j) p[j] = __builtin_amdgcn_exp2f(fmaf(S[j >> 2][qt][j & 3], CSC, -mS[qt]));
      lsum[qt] += ((p[0] + p[1]) + (p[2] + p[3])) + ((p[4] + p[5]) + (p[6] + p[7]));
#pragma unroll
      for (int e = 0; e < 4; ++e) pf[qt][e] = cvtpk(p[2 * e], p[2 * e + 1]);
    }
#pragma unroll
    for (int dh = 0; dh < 2; ++dh) {
      s16x4 lo[4], hi[4];
#pragma unroll
      for (int i = 0; i < 4; ++i) {
        const unsigned a = (va + vs_off) ^ (unsigned)(32 * (dh * 4 + i));
        asm volatile("ds_read_b64_tr_b16 %0, %1" : "=&v"(lo[i]) : "v"(a) : "memory");
        asm volatile("ds_read_b64_tr_b16 %0, %1 offset:4096" : "=&v"(hi[i]) : "v"(a) : "memory");
      }
      asm volatile("s_waitcnt lgkmcnt(0)" : "+v"(lo[0]), "+v"(lo[1]), "+v"(lo[2]), "+v"(lo[3]), "+v"(hi[0]), "+v"(hi[1]), "+v"(hi[2]), "+v"(hi[3]) :: "memory");
      SB0();
#pragma unroll
      for (int i = 0; i < 4; ++i) {
        const u32x2 l2 = __builtin_bit_cast(u32x2, lo[i]), h2 = __builtin_bit_cast(u32x2, hi[i]);
        const u32x4 vf = {l2[0], l2[1], h2[0], h2[1]};
#pragma unroll
        for (int qt = 0; qt < 3; ++qt) oacc[dh * 4 + i][qt] = mfma(vf, pf[qt], oacc[dh * 4 + i][qt]);
      }
    }
  }

  int tz = threadIdx.x; asm volatile("" : "+v"(tz));
  const int lane2 = tz & 63, li2 = lane2 & 15, g2 = lane2 >> 4;
#define lane lane2
#define li li2
#define g g2
  unsigned xb[4];
#pragma unroll
  for (int bq = 0; bq < 4; ++bq) xb[bq] = (unsigned)(li * 1024 + 16 * ((4 * bq + g) ^ li));
  u32x4 w0[8], w1[8];
  const __amdgpu_buffer_rsrc_t rsWo = mk_rsrc(wo, 512 * DM * 2);
  const unsigned voffo = (unsigned)(8 * g * 1024 + h * 256 + 16 * li);
  if (wid < 4) {
    load_w<1024>(w0, rsWo, voffo, 0);
    load_w<1024>(w1, rsWo, voffo, 1);
  }
#pragma unroll
  for (int qt = 0; qt < 3; ++qt) {
    float l = lsum[qt];
    l += __shfl_xor(l, 16);
    l += __shfl_xor(l, 32);
    lsum[qt] = l;
  }
  WAIT_L0();
  bar();
  {
    char* my = lds + RING + wid * 12288;
    const int oth = 1 - kh;
#pragma unroll
    for (int i = 0; i < 4; ++i)
#pragma unroll
      for (int qt = 0; qt < 3; ++qt) {
        const f32x4 v = kh ? oacc[i][qt] : oacc[4 + i][qt];
        *(f32x4*)(my + (i * 3 + qt) * 1024 + lane * 16) = v;
      }
    (void)oth;
    float* ml = (float*)(lds + RING + 8 * 12288) + wid * 384;
#pragma unroll
    for (int qt = 0; qt < 3; ++qt) { ml[(2 * qt) * 64 + lane] = mS[qt]; ml[(2 * qt + 1) * 64 + lane] = lsum[qt]; }
  }
  WAIT_L0();
  bar();
  {
    const int pw = wid ^ 4;
    const char* ot = lds + RING + pw * 12288;
    const float* ml = (const float*)(lds + RING + 8 * 12288) + pw * 384;
#pragma unroll
    for (int qt = 0; qt < 3; ++qt) {
      const float mo = ml[(2 * qt) * 64 + lane], lo_ = ml[(2 * qt + 1) * 64 + lane];
      const float M = fmaxf(mS[qt], mo);
      const float wa = __builtin_amdgcn_exp2f(mS[qt] - M), wb = __builtin_amdgcn_exp2f(mo - M);
      const float inv = 1.0f / (wa * lsum[qt] + wb * lo_);
      const float fa = wa * inv, fb = wb * inv;
      const int row = 16 * qt + li;
#pragma unroll
      for (int i = 0; i < 4; ++i) {
        const f32x4 o = *(const f32x4*)(ot + (i * 3 + qt) * 1024 + lane * 16);
        const f32x4 mine = kh ? oacc[4 + i][qt] : oacc[i][qt];
        const int dt = kh * 4 + i;
        u32x2 r;
        r[0] = cvtpk(mine[0] * fa + o[0] * fb, mine[1] * fa + o[1] * fb);
        r[1] = cvtpk(mine[2] * fa + o[2] * fb, mine[3] * fa + o[3] * fb);
        const int chunk = 16 * h + 2 * dt + (g >> 1);
        *(u32x2*)(lds + row * 1024 + 16 * (chunk ^ li) + 8 * (g & 1)) = r;
      }
    }
  }
  WAIT_L0();
  bar();
  if (wid < 4) {
    f32x4 acc[3][8];
#pragma unroll
    for (int qt = 0; qt < 3; ++qt)
#pragma unroll
      for (int c = 0; c < 8; ++c) acc[qt][c] = f32x4{0.f, 0.f, 0.f, 0.f};
    gemm_main<1024>(acc, w0, w1, rsWo, voffo, lds, xb);
    float* dst = slab + (size_t)blk * SLAB_F + (size_t)wid * 6144 + lane * 4;
#pragma unroll
    for (int qt = 0; qt < 3; ++qt)
#pragma unroll
      for (int cc = 0; cc < 2; ++cc)
#pragma unroll
        for (int r = 0; r < 4; ++r) {
          const f32x4 v = {acc[qt][4 * cc][r], acc[qt][4 * cc + 1][r], acc[qt][4 * cc + 2][r], acc[qt][4 * cc + 3][r]};
          *(f32x4*)(dst + ((qt * 2 + cc) * 4 + r) * 256) = v;
        }
  }
#undef lane
#undef li
#undef g
}

__global__ __launch_bounds__(256) void gqa_reduce_kernel(const float* __restrict__ slab, unsigned short* __restrict__ out) {
  const int idx = blockIdx.x * 256 + threadIdx.x;
  const int lane = idx & 63, r = (idx >> 6) & 3;
  const int t2 = idx >> 8;
  const int rest = t2 / 12, wq3 = t2 % 12, w = wq3 / 3, qt = wq3 % 3;
  const int br = rest >> 4, b = rest & 15;
  const float* src = slab + (size_t)rest * 8 * SLAB_F + (size_t)w * 6144 + (size_t)(qt * 2 * 4 + r) * 256 + lane * 4;
  f32x4 s0 = {0.f, 0.f, 0.f, 0.f}, s1 = {0.f, 0.f, 0.f, 0.f};
#pragma unroll
  for (int j = 0; j < 8; ++j) {
    s0 += *(const f32x4*)(src + (size_t)j * SLAB_F);
    s1 += *(const f32x4*)(src + (size_t)j * SLAB_F + 4 * 256);
  }
  u32x4 o;
  o[0] = cvtpk(s0[0], s0[1]); o[1] = cvtpk(s0[2], s0[3]); o[2] = cvtpk(s1[0], s1[1]); o[3] = cvtpk(s1[2], s1[3]);
  const int q = lane & 15, g = lane >> 4;
  *(u32x4*)(out + ((size_t)(b * SQ + 16 * qt + q) * 1024 + br * 512 + 128 * w + 32 * g + 8 * r)) = o;
}

extern "C" void kernel_launch(void* const* d_in, const int* in_sizes, int n_in,
                              void* d_out, int out_size, void* d_ws, size_t ws_size,
                              hipStream_t stream) {
  (void)in_sizes; (void)n_in; (void)out_size; (void)ws_size;
  hipFuncSetAttribute((const void*)gqa_fused_kernel, hipFuncAttributeMaxDynamicSharedMemorySize, LDS_BYTES);
  float* slab = (float*)d_ws;
  hipLaunchKernelGGL(gqa_fused_kernel, dim3(256), dim3(512), LDS_BYTES, stream,
                     (const unsigned short*)d_in[0], (const unsigned short*)d_in[1], (const unsigned short*)d_in[2],
                     (const unsigned short*)d_in[3], (const unsigned short*)d_in[4],
                     (const unsigned short*)d_in[5], (const unsigned short*)d_in[6], (const unsigned short*)d_in[7],
                     (const unsigned short*)d_in[8], (const unsigned short*)d_in[9], slab);
  hipLaunchKernelGGL(gqa_reduce_kernel, dim3(384), dim3(256), 0, stream, (const float*)slab, (unsigned short*)d_out);
}
```

```cpp
#include <hip/hip_runtime.h>
#include <hip/hip_bf16.h>

typedef __bf16   bf16x8 __attribute__((ext_vector_type(8)));
typedef __bf16   bf16x2 __attribute__((ext_vector_type(2)));
typedef short    s16x4  __attribute__((ext_vector_type(4)));
typedef float    f32x4  __attribute__((ext_vector_type(4)));
typedef float    f32x2  __attribute__((ext_vector_type(2)));
typedef unsigned u32x4  __attribute__((ext_vector_type(4)));
typedef unsigned u32x2  __attribute__((ext_vector_type(2)));
#define LAS __attribute__((address_space(3)))
#define DI __device__ __forceinline__

#define STAMP(i) do {} while (0)
#define STAMP_DECL do {} while (0)
#define STAMP_FLUSH do {} while (0)

namespace {
constexpr int NB = 16, SQ = 48, DM = 512, HQ = 32, HKV = 8, SK = 512, DH = 128;
constexpr int XO_BYTES = SQ * DM * 2;
constexpr int SLOT = 16384, NSLOT = 7;
constexpr int RING = XO_BYTES;
constexpr int LDS_BYTES = XO_BYTES + NSLOT * SLOT;
constexpr float CSC = 0.08838834764831845f * 1.4426950408889634f;
constexpr float THR = 8.0f;
constexpr size_t SLAB_F = (size_t)SQ * DM;

DI unsigned cvtpk(float lo, float hi) {
  f32x2 v = {lo, hi}; bf16x2 b = __builtin_convertvector(v, bf16x2); return __builtin_bit_cast(unsigned, b);
}
DI f32x4 mfma(u32x4 a, u32x4 b, f32x4 c) {
  return __builtin_amdgcn_mfma_f32_16x16x32_bf16(__builtin_bit_cast(bf16x8, a), __builtin_bit_cast(bf16x8, b), c, 0, 0, 0);
}
DI void bar() { __builtin_amdgcn_s_barrier(); }
#define WAIT_L0() asm volatile("s_waitcnt lgkmcnt(0)" ::: "memory")
#define WAIT_V(n) asm volatile("s_waitcnt vmcnt(" #n ")" ::: "memory")
#define SB0() __builtin_amdgcn_sched_barrier(0)

DI __amdgpu_buffer_rsrc_t mk_rsrc(const void* p, unsigned bytes) {
  return __builtin_amdgcn_make_buffer_rsrc((void*)p, (short)0, (int)bytes, 0x00020000);
}
DI void dma16(__amdgpu_buffer_rsrc_t rs, char* lds_dst, unsigned voff, unsigned soff) {
  __builtin_amdgcn_raw_ptr_buffer_load_lds(rs, (LAS void*)lds_dst, 16, (int)voff, (int)soff, 0, 0);
}

template <unsigned WSTRIDE>
DI void load_w(u32x4 (&w)[8], __amdgpu_buffer_rsrc_t rs, unsigned voff, int ks) {
#pragma unroll
  for (int j = 0; j < 8; ++j)
    w[j] = __builtin_amdgcn_raw_buffer_load_b128(rs, (int)voff, (int)((unsigned)(32 * ks + j) * WSTRIDE), 0);
}
DI void perm_d(u32x4& f0, u32x4& f1, const u32x4 (&w)[8], int d) {
#pragma unroll
  for (int p = 0; p < 4; ++p) {
    f0[p] = __builtin_amdgcn_perm(w[2 * p + 1][d], w[2 * p][d], 0x05040100u);
    f1[p] = __builtin_amdgcn_perm(w[2 * p + 1][d], w[2 * p][d], 0x07060302u);
  }
}
DI void mma_d(f32x4 (&acc)[3][8], const u32x4& f0, const u32x4& f1, const u32x4 (&xb)[3], int d) {
#pragma unroll
  for (int qt = 0; qt < 3; ++qt) {
    acc[qt][2 * d]     = mfma(f0, xb[qt], acc[qt][2 * d]);
    acc[qt][2 * d + 1] = mfma(f1, xb[qt], acc[qt][2 * d + 1]);
  }
}
DI void read_x(u32x4 (&xb)[3], const char* lds, unsigned addr) {
#pragma unroll
  for (int qt = 0; qt < 3; ++qt) xb[qt] = *(const u32x4*)(lds + addr + qt * 16384);
}
template <unsigned WSTRIDE, int I>
DI void gemm_step(f32x4 (&acc)[3][8], u32x4 (&w)[8], u32x4 (&fa)[2], u32x4 (&fb)[2], u32x4 (&xc)[3], u32x4 (&xn)[3],
                  __amdgpu_buffer_rsrc_t rs, unsigned voff, const char* lds, unsigned xbase, int rot) {
  perm_d(fa[0], fa[1], w, 0);
  if (I > 0) mma_d(acc, fb[0], fb[1], xn, 3);
  SB0();
  if (I + 1 < 16) read_x(xn, lds, xbase ^ (unsigned)(((I + 1 + rot) & 15) << 6));
  perm_d(fb[0], fb[1], w, 1);
  mma_d(acc, fa[0], fa[1], xc, 0);
  SB0();
  perm_d(fa[0], fa[1], w, 2);
  mma_d(acc, fb[0], fb[1], xc, 1);
  SB0();
  perm_d(fb[0], fb[1], w, 3);
  mma_d(acc, fa[0], fa[1], xc, 2);
  SB0();
  if (I + 3 < 16) load_w<WSTRIDE>(w, rs, voff, (I + 3 + rot) & 15);
  SB0();
  if (I == 15) mma_d(acc, fb[0], fb[1], xc, 3);
}
template <unsigned WSTRIDE>
DI void gemm_main(f32x4 (&acc)[3][8], u32x4 (&w0)[8], u32x4 (&w1)[8], u32x4 (&w2)[8], __amdgpu_buffer_rsrc_t rs, unsigned voff, const char* lds, unsigned xbase, int rot) {
  u32x4 fa[2], fb[2], x0[3], x1[3];
  read_x(x0, lds, xbase ^ (unsigned)(rot << 6));
#define GSA(i, W) gemm_step<WSTRIDE, i>(acc, W, fa, fb, x0, x1, rs, voff, lds, xbase, rot);
#define GSB(i, W) gemm_step<WSTRIDE, i>(acc, W, fa, fb, x1, x0, rs, voff, lds, xbase, rot);
  GSA(0, w0) GSB(1, w1) GSA(2, w2) GSB(3, w0) GSA(4, w1) GSB(5, w2) GSA(6, w0) GSB(7, w1)
  GSA(8, w2) GSB(9, w0) GSA(10, w1) GSB(11, w2) GSA(12, w0) GSB(13, w1) GSA(14, w2) GSB(15, w0)
#undef GSA
#undef GSB
}
}

__global__ __launch_bounds__(512, 2) void gqa_fused_kernel(
    const unsigned short* __restrict__ xA, const unsigned short* __restrict__ kA, const unsigned short* __restrict__ vA,
    const unsigned short* __restrict__ wqA, const unsigned short* __restrict__ woA,
    const unsigned short* __restrict__ xB, const unsigned short* __restrict__ kB, const unsigned short* __restrict__ vB,
    const unsigned short* __restrict__ wqB, const unsigned short* __restrict__ woB,
    float* __restrict__ slab) {
  extern __shared__ __attribute__((aligned(16))) char lds[];
  STAMP_DECL;
  STAMP(0);
  const int tid = threadIdx.x, lane = tid & 63;
  const int wid = __builtin_amdgcn_readfirstlane(tid >> 6);
  const int li = lane & 15, g = lane >> 4;
  const int blk = blockIdx.x, kvh = blk & 7, rest = blk >> 3, br = rest >> 4, b = rest & 15;
  const unsigned short* x  = (br ? xB : xA) + (size_t)b * SQ * DM;
  const unsigned short* kp = (br ? kB : kA) + ((size_t)b * HKV + kvh) * SK * DH;
  const unsigned short* vp = (br ? vB : vA) + ((size_t)b * HKV + kvh) * SK * DH;
  const unsigned short* wq = (br ? wqB : wqA) + (size_t)kvh * 512;
  const unsigned short* wo = (br ? woB : woA) + (size_t)kvh * 512 * DM;
  const __amdgpu_buffer_rsrc_t rsK = mk_rsrc(kp, SK * DH * 2), rsV = mk_rsrc(vp, SK * DH * 2);

  const unsigned xbase = (unsigned)(li * 1024 + 16 * (g ^ li));
  const int rot = b;

  const int h = wid & 3, kh = wid >> 2;
  u32x4 qf[3][4];

  auto dma_tile = [&](int u, int p0, int np) {
    char* dst = lds + RING + (u % NSLOT) * SLOT;
    const int t = u >> 1;
#pragma unroll
    for (int i = 0; i < np; ++i) {
      const int p = p0 + i;
      const int row = 4 * p + g;
      if (u & 1) dma16(rsV, dst + p * 1024, (unsigned)(g * 256 + 16 * (li ^ (2 * (row & 7)))), (unsigned)((t * 64 + 4 * p) * 256));
      else       dma16(rsK, dst + p * 1024, (unsigned)(g * 256 + 16 * (li ^ (row & 15))), (unsigned)((t * 64 + 4 * p) * 256));
    }
  };

  if (wid >= 4) {
    const __amdgpu_buffer_rsrc_t rsX = mk_rsrc(x, SQ * DM * 2);
    const int w4 = wid - 4;
#pragma unroll
    for (int i = 0; i < 12; ++i) {
      const int row = w4 * 12 + i;
      dma16(rsX, lds + row * 1024, (unsigned)(16 * (lane ^ (row & 15))), (unsigned)(row * 1024));
    }
    STAMP(1);
    WAIT_V(0);
    STAMP(2);
    bar();
#pragma unroll
    for (int u = 0; u < NSLOT; ++u) dma_tile(u, w4 * 4, 4);
    bar();
    STAMP(3);
  } else {
    const __amdgpu_buffer_rsrc_t rsW = mk_rsrc(wq, (unsigned)(DM * HQ * DH * 2 - kvh * 1024));
    const int cg = 4 * (li & 3) + (li >> 2);
    const unsigned voff = (unsigned)(8 * g * 8192 + wid * 256 + 16 * cg);
    u32x4 w0[8], w1[8], w2[8];
    load_w<8192>(w0, rsW, voff, rot);
    load_w<8192>(w1, rsW, voff, (rot + 1) & 15);
    load_w<8192>(w2, rsW, voff, (rot + 2) & 15);
    f32x4 acc[3][8];
#pragma unroll
    for (int qt = 0; qt < 3; ++qt)
#pragma unroll
      for (int c = 0; c < 8; ++c) acc[qt][c] = f32x4{0.f, 0.f, 0.f, 0.f};
    STAMP(1);
    bar();
    STAMP(2);
    gemm_main<8192>(acc, w0, w1, w2, rsW, voff, lds, xbase, rot);
    STAMP(3);
#pragma unroll
    for (int qt = 0; qt < 3; ++qt)
#pragma unroll
      for (int s = 0; s < 4; ++s) {
        u32x4 f;
#pragma unroll
        for (int p = 0; p < 4; ++p) f[p] = cvtpk(acc[qt][2 * p][s] * CSC, acc[qt][2 * p + 1][s] * CSC);
        qf[qt][s] = f;
      }
    WAIT_L0();
    bar();
#pragma unroll
    for (int qt = 0; qt < 3; ++qt)
#pragma unroll
      for (int s = 0; s < 4; ++s) *(u32x4*)(lds + wid * 12288 + (qt * 4 + s) * 1024 + lane * 16) = qf[qt][s];
    WAIT_L0();
  }

  unsigned ka[4];
#pragma unroll
  for (int s = 0; s < 4; ++s) ka[s] = (unsigned)(RING + (kh * 32 + li) * 256 + 16 * ((4 * s + g) ^ li));
  const int vr = 4 * g + (li >> 2);
  const unsigned va = (unsigned)(RING + (kh * 32 + vr) * 256 + 32 * (vr & 7) + 8 * (li & 3)) ^ (unsigned)(128 * kh);

  unsigned dko[2], dvo[2];
#pragma unroll
  for (int i = 0; i < 2; ++i) {
    const int row = 8 * wid + 4 * i + g;
    dko[i] = (unsigned)(g * 256 + 16 * (li ^ (row & 15)));
    dvo[i] = (unsigned)(g * 256 + 16 * (li ^ (2 * (row & 7))));
  }
  f32x4 oacc[8][3];
#pragma unroll
  for (int dt = 0; dt < 8; ++dt)
#pragma unroll
    for (int qt = 0; qt < 3; ++qt) oacc[dt][qt] = f32x4{0.f, 0.f, 0.f, 0.f};
  float mS[3], lsum[3];
  f32x4 negm[3];
#pragma unroll
  for (int qt = 0; qt < 3; ++qt) { mS[qt] = 0.f; lsum[qt] = 0.f; negm[qt] = f32x4{0.f, 0.f, 0.f, 0.f}; }

  auto qk = [&](f32x4 (&S)[2][3], unsigned ks_off) {
#pragma unroll
    for (int kt = 0; kt < 2; ++kt) {
#pragma unroll
      for (int s = 0; s < 4; ++s) {
        const u32x4 kf = *(const u32x4*)(lds + ka[s] + ks_off + kt * 4096);
#pragma unroll
        for (int qt = 0; qt < 3; ++qt) S[kt][qt] = mfma(kf, qf[qt][s], s == 0 ? negm[qt] : S[kt][qt]);
      }
    }
  };
  auto remax = [&](f32x4 (&S)[2][3], bool first) {
#pragma unroll
    for (int qt = 0; qt < 3; ++qt) {
      float m = fmaxf(fmaxf(fmaxf(S[0][qt][0] + 0.f, S[0][qt][1] + 0.f), fmaxf(S[0][qt][2] + 0.f, S[0][qt][3] + 0.f)),
                      fmaxf(fmaxf(S[1][qt][0] + 0.f, S[1][qt][1] + 0.f), fmaxf(S[1][qt][2] + 0.f, S[1][qt][3] + 0.f)));
      m = fmaxf(m, __shfl_xor(m, 16));
      m = fmaxf(m, __shfl_xor(m, 32));
      const float dl = first ? m : fmaxf(m, 0.f);
      mS[qt] += dl;
      negm[qt] = f32x4{-mS[qt], -mS[qt], -mS[qt], -mS[qt]};
      S[0][qt] -= dl; S[1][qt] -= dl;
      if (!first) {
        const float al = __builtin_amdgcn_exp2f(-dl);
        lsum[qt] *= al;
#pragma unroll
        for (int dt = 0; dt < 8; ++dt) oacc[dt][qt] *= al;
      }
    }
  };
  auto exp_sum = [&](f32x4 (&S)[2][3], float (&ps)[3]) {
#pragma unroll
    for (int qt = 0; qt < 3; ++qt) {
#pragma unroll
      for (int j = 0; j < 8; ++j) S[j >> 2][qt][j & 3] = __builtin_amdgcn_exp2f(S[j >> 2][qt][j & 3]);
      float a = S[0][qt][0];
#pragma unroll
      for (int j = 1; j < 8; ++j) a += S[j >> 2][qt][j & 3];
      ps[qt] = a;
    }
  };
  auto pack = [&](u32x4 (&pf)[3], const f32x4 (&P)[2][3]) {
#pragma unroll
    for (int qt = 0; qt < 3; ++qt)
#pragma unroll
      for (int e = 0; e < 4; ++e) pf[qt][e] = cvtpk(P[e >> 1][qt][2 * (e & 1)], P[e >> 1][qt][2 * (e & 1) + 1]);
  };
  auto pv = [&](const u32x4 (&pf)[3], unsigned vs_off) {
#pragma unroll
    for (int dh = 0; dh < 2; ++dh) {
      s16x4 lo[4], hi[4];
#pragma unroll
      for (int i = 0; i < 4; ++i) {
        const unsigned a = (va + vs_off) ^ (unsigned)(32 * (dh * 4 + i));
        asm volatile("ds_read_b64_tr_b16 %0, %1" : "=&v"(lo[i]) : "v"(a) : "memory");
        asm volatile("ds_read_b64_tr_b16 %0, %1 offset:4096" : "=&v"(hi[i]) : "v"(a) : "memory");
      }
      asm volatile("s_waitcnt lgkmcnt(0)" : "+v"(lo[0]), "+v"(lo[1]), "+v"(lo[2]), "+v"(lo[3]), "+v"(hi[0]), "+v"(hi[1]), "+v"(hi[2]), "+v"(hi[3]) :: "memory");
#pragma unroll
      for (int i = 0; i < 4; ++i) {
        const u32x2 l2 = __builtin_bit_cast(u32x2, lo[i]), h2 = __builtin_bit_cast(u32x2, hi[i]);
        const u32x4 vf = {l2[0], l2[1], h2[0], h2[1]};
#pragma unroll
        for (int qt = 0; qt < 3; ++qt) oacc[dh * 4 + i][qt] = mfma(vf, pf[qt], oacc[dh * 4 + i][qt]);
      }
    }
  };

  f32x4 P[2][3];
  WAIT_V(24);
  WAIT_L0();
  bar();
  if (wid >= 4) {
#pragma unroll
    for (int qt = 0; qt < 3; ++qt)
#pragma unroll
      for (int s = 0; s < 4; ++s) qf[qt][s] = *(const u32x4*)(lds + h * 12288 + (qt * 4 + s) * 1024 + lane * 16);
  }
  {
    float ps[3];
    qk(P, 0u);
    remax(P, true);
    exp_sum(P, ps);
#pragma unroll
    for (int qt = 0; qt < 3; ++qt) lsum[qt] = ps[qt];
  }
  STAMP(4);
  int slV = 1;
#pragma nounroll
  for (int t = 0; t < 7; ++t) {
    if (t == 0) WAIT_V(16); else if (t == 1) WAIT_V(10); else if (t <= 5) WAIT_V(6); else WAIT_V(2);
    WAIT_L0();
    bar();
    const int slK = slV == 6 ? 0 : slV + 1;
    if (t <= 4) {
      const int fK = slV >= 2 ? slV - 2 : slV + 5, fV = slV >= 1 ? slV - 1 : 6;
      if (t >= 1) {
        char* d1 = lds + RING + fK * SLOT + wid * 2048;
        dma16(rsK, d1, dko[0], (unsigned)(((t + 3) * 64 + 8 * wid) * 256));
        dma16(rsK, d1 + 1024, dko[1], (unsigned)(((t + 3) * 64 + 8 * wid + 4) * 256));
      }
      char* d0 = lds + RING + fV * SLOT + wid * 2048;
      dma16(rsV, d0, dvo[0], (unsigned)(((t + 3) * 64 + 8 * wid) * 256));
      dma16(rsV, d0 + 1024, dvo[1], (unsigned)(((t + 3) * 64 + 8 * wid + 4) * 256));
    }
    f32x4 S[2][3];
    u32x4 pf[3];
    float ps[3];
    pack(pf, P);
    qk(S, (unsigned)(slK * SLOT));
    pv(pf, (unsigned)(slV * SLOT));
    exp_sum(S, ps);
    if (__any((ps[0] > 65536.f) | (ps[1] > 65536.f) | (ps[2] > 65536.f))) {
      qk(S, (unsigned)(slK * SLOT));
      remax(S, false);
      exp_sum(S, ps);
    }
#pragma unroll
    for (int qt = 0; qt < 3; ++qt) { lsum[qt] += ps[qt]; P[0][qt] = S[0][qt]; P[1][qt] = S[1][qt]; }
    slV = slV >= 5 ? slV - 5 : slV + 2;
  }
  {
    WAIT_V(0);
    WAIT_L0();
    bar();
    STAMP(11);
    u32x4 pf[3];
    pack(pf, P);
    pv(pf, (unsigned)(slV * SLOT));
  }

  STAMP(12);
  int tz = threadIdx.x; asm volatile("" : "+v"(tz));
  const int lane2 = tz & 63, li2 = lane2 & 15, g2 = lane2 >> 4;
#define lane lane2
#define li li2
#define g g2
  const unsigned xbase2 = (unsigned)(li * 1024 + 16 * (g ^ li));
  u32x4 w0[8], w1[8], w2[8];
  const __amdgpu_buffer_rsrc_t rsWo = mk_rsrc(wo, 512 * DM * 2);
  const unsigned voffo = (unsigned)(8 * g * 1024 + h * 256 + 16 * li);
  if (wid < 4) {
    load_w<1024>(w0, rsWo, voffo, rot);
    load_w<1024>(w1, rsWo, voffo, (rot + 1) & 15);
  }
#pragma unroll
  for (int qt = 0; qt < 3; ++qt) {
    float l = lsum[qt];
    l += __shfl_xor(l, 16);
    l += __shfl_xor(l, 32);
    lsum[qt] = l;
  }
  WAIT_L0();
  bar();
  {
    char* my = lds + RING + wid * 12288;
#pragma unroll
    for (int i = 0; i < 4; ++i)
#pragma unroll
      for (int qt = 0; qt < 3; ++qt) *(f32x4*)(my + (i * 3 + qt) * 1024 + lane * 16) = oacc[4 + i][qt];
    float* ml = (float*)(lds + RING + 8 * 12288) + wid * 384;
#pragma unroll
    for (int qt = 0; qt < 3; ++qt) { ml[(2 * qt) * 64 + lane] = mS[qt]; ml[(2 * qt + 1) * 64 + lane] = lsum[qt]; }
  }
  WAIT_L0();
  bar();
  {
    const int pw = wid ^ 4;
    const char* ot = lds + RING + pw * 12288;
    const float* ml = (const float*)(lds + RING + 8 * 12288) + pw * 384;
    float mo[3], lo_[3];
    f32x4 o[4][3];
#pragma unroll
    for (int qt = 0; qt < 3; ++qt) { mo[qt] = ml[(2 * qt) * 64 + lane]; lo_[qt] = ml[(2 * qt + 1) * 64 + lane]; }
#pragma unroll
    for (int i = 0; i < 4; ++i)
#pragma unroll
      for (int qt = 0; qt < 3; ++qt) o[i][qt] = *(const f32x4*)(ot + (i * 3 + qt) * 1024 + lane * 16);
    SB0();
#pragma unroll
    for (int qt = 0; qt < 3; ++qt) {
      const float M = fmaxf(mS[qt], mo[qt]);
      const float wa = __builtin_amdgcn_exp2f(mS[qt] - M), wb = __builtin_amdgcn_exp2f(mo[qt] - M);
      const float inv = __builtin_amdgcn_rcpf(wa * lsum[qt] + wb * lo_[qt]);
      const float fa = wa * inv, fb = wb * inv;
      const int row = 16 * qt + li;
#pragma unroll
      for (int i = 0; i < 4; ++i) {
        const f32x4 mine = oacc[i][qt];
        const int dt = kh * 4 + i;
        u32x2 r;
        r[0] = cvtpk(mine[0] * fa + o[i][qt][0] * fb, mine[1] * fa + o[i][qt][1] * fb);
        r[1] = cvtpk(mine[2] * fa + o[i][qt][2] * fb, mine[3] * fa + o[i][qt][3] * fb);
        const int chunk = 16 * h + 2 * dt + (g >> 1);
        *(u32x2*)(lds + row * 1024 + 16 * (chunk ^ li) + 8 * (g & 1)) = r;
      }
    }
  }
  if (wid < 4) load_w<1024>(w2, rsWo, voffo, (rot + 2) & 15);
  WAIT_L0();
  bar();
  STAMP(13);
  if (wid < 4) {
    f32x4 acc[3][8];
#pragma unroll
    for (int qt = 0; qt < 3; ++qt)
#pragma unroll
      for (int c = 0; c < 8; ++c) acc[qt][c] = f32x4{0.f, 0.f, 0.f, 0.f};
    gemm_main<1024>(acc, w0, w1, w2, rsWo, voffo, lds, xbase2, rot);
    STAMP(14);
    float* dst = slab + (size_t)blk * SLAB_F + (size_t)wid * 6144 + lane * 4;
#pragma unroll
    for (int qt = 0; qt < 3; ++qt)
#pragma unroll
      for (int cc = 0; cc < 2; ++cc)
#pragma unroll
        for (int r = 0; r < 4; ++r) {
          const f32x4 v = {acc[qt][4 * cc][r], acc[qt][4 * cc + 1][r], acc[qt][4 * cc + 2][r], acc[qt][4 * cc + 3][r]};
          *(f32x4*)(dst + ((qt * 2 + cc) * 4 + r) * 256) = v;
        }
  }
  STAMP(15);
  STAMP_FLUSH;
#undef lane
#undef li
#undef g
}

__global__ __launch_bounds__(256) void gqa_reduce_kernel(const float* __restrict__ slab, unsigned short* __restrict__ out) {
  const int idx = blockIdx.x * 256 + threadIdx.x;
  const int lane = idx & 63, r = (idx >> 6) & 3;
  const int t2 = idx >> 8;
  const int rest = t2 / 12, wq3 = t2 % 12, w = wq3 / 3, qt = wq3 % 3;
  const int br = rest >> 4, b = rest & 15;
  const float* src = slab + (size_t)rest * 8 * SLAB_F + (size_t)w * 6144 + (size_t)(qt * 2 * 4 + r) * 256 + lane * 4;
  f32x4 s0 = {0.f, 0.f, 0.f, 0.f}, s1 = {0.f, 0.f, 0.f, 0.f};
#pragma unroll
  for (int j = 0; j < 8; ++j) {
    s0 += *(const f32x4*)(src + (size_t)j * SLAB_F);
    s1 += *(const f32x4*)(src + (size_t)j * SLAB_F + 4 * 256);
  }
  u32x4 o;
  o[0] = cvtpk(s0[0], s0[1]); o[1] = cvtpk(s0[2], s0[3]); o[2] = cvtpk(s1[0], s1[1]); o[3] = cvtpk(s1[2], s1[3]);
  const int q = lane & 15, g = lane >> 4;
  *(u32x4*)(out + ((size_t)(b * SQ + 16 * qt + q) * 1024 + br * 512 + 128 * w + 32 * g + 8 * r)) = o;
}

extern "C" void kernel_launch(void* const* d_in, const int* in_sizes, int n_in,
                              void* d_out, int out_size, void* d_ws, size_t ws_size,
                              hipStream_t stream) {
  (void)in_sizes; (void)n_in; (void)out_size; (void)ws_size;
  hipFuncSetAttribute((const void*)gqa_fused_kernel, hipFuncAttributeMaxDynamicSharedMemorySize, LDS_BYTES);
  float* slab = (float*)d_ws;
  hipLaunchKernelGGL(gqa_fused_kernel, dim3(256), dim3(512), LDS_BYTES, stream,
                     (const unsigned short*)d_in[0], (const unsigned short*)d_in[1], (const unsigned short*)d_in[2],
                     (const unsigned short*)d_in[3], (const unsigned short*)d_in[4],
                     (const unsigned short*)d_in[5], (const unsigned short*)d_in[6], (const unsigned short*)d_in[7],
                     (const unsigned short*)d_in[8], (const unsigned short*)d_in[9], slab);
  hipLaunchKernelGGL(gqa_reduce_kernel, dim3(384), dim3(256), 0, stream, (const float*)slab, (unsigned short*)d_out);
}
```

```cpp
#include <hip/hip_runtime.h>
#include <hip/hip_bf16.h>

typedef __bf16   bf16x8 __attribute__((ext_vector_type(8)));
typedef __bf16   bf16x2 __attribute__((ext_vector_type(2)));
typedef short    s16x4  __attribute__((ext_vector_type(4)));
typedef float    f32x4  __attribute__((ext_vector_type(4)));
typedef float    f32x2  __attribute__((ext_vector_type(2)));
typedef unsigned u32x4  __attribute__((ext_vector_type(4)));
typedef unsigned u32x2  __attribute__((ext_vector_type(2)));
#define LAS __attribute__((address_space(3)))
#define DI __device__ __forceinline__

#define STAMP(i) do {} while (0)
#define STAMP_DECL do {} while (0)
#define STAMP_FLUSH do {} while (0)

namespace {
constexpr int NB = 16, SQ = 48, DM = 512, HQ = 32, HKV = 8, SK = 512, DH = 128;
constexpr int XO_BYTES = SQ * DM * 2;
constexpr int SLOT = 16384, NSLOT = 7;
constexpr int RING = XO_BYTES;
constexpr int LDS_BYTES = XO_BYTES + NSLOT * SLOT;
constexpr float CSC = 0.08838834764831845f * 1.4426950408889634f;
constexpr float THR = 8.0f;
constexpr size_t SLAB_F = (size_t)SQ * DM;

DI unsigned cvtpk(float lo, float hi) {
  f32x2 v = {lo, hi}; bf16x2 b = __builtin_convertvector(v, bf16x2); return __builtin_bit_cast(unsigned, b);
}
DI f32x4 mfma(u32x4 a, u32x4 b, f32x4 c) {
  return __builtin_amdgcn_mfma_f32_16x16x32_bf16(__builtin_bit_cast(bf16x8, a), __builtin_bit_cast(bf16x8, b), c, 0, 0, 0);
}
DI void bar() { __builtin_amdgcn_s_barrier(); }
#define WAIT_L0() asm volatile("s_waitcnt lgkmcnt(0)" ::: "memory")
#define WAIT_V(n) asm volatile("s_waitcnt vmcnt(" #n ")" ::: "memory")
#define SB0() __builtin_amdgcn_sched_barrier(0)

DI __amdgpu_buffer_rsrc_t mk_rsrc(const void* p, unsigned bytes) {
  return __builtin_amdgcn_make_buffer_rsrc((void*)p, (short)0, (int)bytes, 0x00020000);
}
DI void dma16(__amdgpu_buffer_rsrc_t rs, char* lds_dst, unsigned voff, unsigned soff) {
  __builtin_amdgcn_raw_ptr_buffer_load_lds(rs, (LAS void*)lds_dst, 16, (int)voff, (int)soff, 0, 0);
}

template <unsigned WSTRIDE>
DI void load_w(u32x4 (&w)[8], __amdgpu_buffer_rsrc_t rs, unsigned voff, int ks) {
#pragma unroll
  for (int j = 0; j < 8; ++j)
    w[j] = __builtin_amdgcn_raw_buffer_load_b128(rs, (int)voff, (int)((unsigned)(32 * ks + j) * WSTRIDE), 0);
}
DI void perm_d(u32x4& f0, u32x4& f1, const u32x4 (&w)[8], int d) {
#pragma unroll
  for (int p = 0; p < 4; ++p) {
    f0[p] = __builtin_amdgcn_perm(w[2 * p + 1][d], w[2 * p][d], 0x05040100u);
    f1[p] = __builtin_amdgcn_perm(w[2 * p + 1][d], w[2 * p][d], 0x07060302u);
  }
}
DI void mma_d(f32x4 (&acc)[3][8], const u32x4& f0, const u32x4& f1, const u32x4 (&xb)[3], int d) {
#pragma unroll
  for (int qt = 0; qt < 3; ++qt) {
    acc[qt][2 * d]     = mfma(f0, xb[qt], acc[qt][2 * d]);
    acc[qt][2 * d + 1] = mfma(f1, xb[qt], acc[qt][2 * d + 1]);
  }
}
DI void read_x(u32x4 (&xb)[3], const char* lds, unsigned addr) {
#pragma unroll
  for (int qt = 0; qt < 3; ++qt) xb[qt] = *(const u32x4*)(lds + addr + qt * 16384);
}
template <unsigned WSTRIDE, int I>
DI void gemm_step(f32x4 (&acc)[3][8], u32x4 (&w)[8], u32x4 (&fa)[2], u32x4 (&fb)[2], u32x4 (&xc)[3], u32x4 (&xn)[3],
                  __amdgpu_buffer_rsrc_t rs, unsigned voff, const char* lds, unsigned xbase, int rot) {
  perm_d(fa[0], fa[1], w, 0);
  if (I > 0) mma_d(acc, fb[0], fb[1], xn, 3);
  SB0();
  if (I + 1 < 16) read_x(xn, lds, xbase ^ (unsigned)(((I + 1 + rot) & 15) << 6));
  perm_d(fb[0], fb[1], w, 1);
  mma_d(acc, fa[0], fa[1], xc, 0);
  SB0();
  perm_d(fa[0], fa[1], w, 2);
  mma_d(acc, fb[0], fb[1], xc, 1);
  SB0();
  perm_d(fb[0], fb[1], w, 3);
  mma_d(acc, fa[0], fa[1], xc, 2);
  SB0();
  if (I + 3 < 16) load_w<WSTRIDE>(w, rs, voff, (I + 3 + rot) & 15);
  SB0();
  if (I == 15) mma_d(acc, fb[0], fb[1], xc, 3);
}
template <unsigned WSTRIDE>
DI void gemm_main(f32x4 (&acc)[3][8], u32x4 (&w0)[8], u32x4 (&w1)[8], u32x4 (&w2)[8], __amdgpu_buffer_rsrc_t rs, unsigned voff, const char* lds, unsigned xbase, int rot) {
  u32x4 fa[2], fb[2], x0[3], x1[3];
  read_x(x0, lds, xbase ^ (unsigned)(rot << 6));
#define GSA(i, W) gemm_step<WSTRIDE, i>(acc, W, fa, fb, x0, x1, rs, voff, lds, xbase, rot);
#define GSB(i, W) gemm_step<WSTRIDE, i>(acc, W, fa, fb, x1, x0, rs, voff, lds, xbase, rot);
  GSA(0, w0) GSB(1, w1) GSA(2, w2) GSB(3, w0) GSA(4, w1) GSB(5, w2) GSA(6, w0) GSB(7, w1)
  GSA(8, w2) GSB(9, w0) GSA(10, w1) GSB(11, w2) GSA(12, w0) GSB(13, w1) GSA(14, w2) GSB(15, w0)
#undef GSA
#undef GSB
}
}

__global__ __launch_bounds__(512, 2) void gqa_fused_kernel(
    const unsigned short* __restrict__ xA, const unsigned short* __restrict__ kA, const unsigned short* __restrict__ vA,
    const unsigned short* __restrict__ wqA, const unsigned short* __restrict__ woA,
    const unsigned short* __restrict__ xB, const unsigned short* __restrict__ kB, const unsigned short* __restrict__ vB,
    const unsigned short* __restrict__ wqB, const unsigned short* __restrict__ woB,
    float* __restrict__ slab) {
  extern __shared__ __attribute__((aligned(16))) char lds[];
  STAMP_DECL;
  STAMP(0);
  const int tid = threadIdx.x, lane = tid & 63;
  const int wid = __builtin_amdgcn_readfirstlane(tid >> 6);
  const int li = lane & 15, g = lane >> 4;
  const int blk = blockIdx.x, kvh = blk & 7, rest = blk >> 3, br = rest >> 4, b = rest & 15;
  const unsigned short* x  = (br ? xB : xA) + (size_t)b * SQ * DM;
  const unsigned short* kp = (br ? kB : kA) + ((size_t)b * HKV + kvh) * SK * DH;
  const unsigned short* vp = (br ? vB : vA) + ((size_t)b * HKV + kvh) * SK * DH;
  const unsigned short* wq = (br ? wqB : wqA) + (size_t)kvh * 512;
  const unsigned short* wo = (br ? woB : woA) + (size_t)kvh * 512 * DM;
  const __amdgpu_buffer_rsrc_t rsK = mk_rsrc(kp, SK * DH * 2), rsV = mk_rsrc(vp, SK * DH * 2);

  const unsigned xbase = (unsigned)(li * 1024 + 16 * (g ^ li));
  const int rot = b;

  const int h = wid & 3, kh = wid >> 2;
  u32x4 qf[3][4];

  auto dma_tile = [&](int u, int p0, int np) {
    char* dst = lds + RING + (u % NSLOT) * SLOT;
    const int t = u >> 1;
#pragma unroll
    for (int i = 0; i < np; ++i) {
      const int p = p0 + i;
      const int row = 4 * p + g;
      if (u & 1) dma16(rsV, dst + p * 1024, (unsigned)(g * 256 + 16 * (li ^ (2 * (row & 7)))), (unsigned)((t * 64 + 4 * p) * 256));
      else       dma16(rsK, dst + p * 1024, (unsigned)(g * 256 + 16 * (li ^ (row & 15))), (unsigned)((t * 64 + 4 * p) * 256));
    }
  };

  if (wid >= 4) {
    const __amdgpu_buffer_rsrc_t rsX = mk_rsrc(x, SQ * DM * 2);
    const int w4 = wid - 4;
#pragma unroll
    for (int i = 0; i < 12; ++i) {
      const int row = w4 * 12 + i;
      dma16(rsX, lds + row * 1024, (unsigned)(16 * (lane ^ (row & 15))), (unsigned)(row * 1024));
    }
    STAMP(1);
    WAIT_V(0);
    STAMP(2);
    bar();
#pragma unroll
    for (int u = 0; u < NSLOT; ++u) dma_tile(u, w4 * 4, 4);
    bar();
    STAMP(3);
  } else {
    const __amdgpu_buffer_rsrc_t rsW = mk_rsrc(wq, (unsigned)(DM * HQ * DH * 2 - kvh * 1024));
    const int cg = 4 * (li & 3) + (li >> 2);
    const unsigned voff = (unsigned)(8 * g * 8192 + wid * 256 + 16 * cg);
    u32x4 w0[8], w1[8], w2[8];
    load_w<8192>(w0, rsW, voff, rot);
    load_w<8192>(w1, rsW, voff, (rot + 1) & 15);
    load_w<8192>(w2, rsW, voff, (rot + 2) & 15);
    f32x4 acc[3][8];
#pragma unroll
    for (int qt = 0; qt < 3; ++qt)
#pragma unroll
      for (int c = 0; c < 8; ++c) acc[qt][c] = f32x4{0.f, 0.f, 0.f, 0.f};
    STAMP(1);
    bar();
    STAMP(2);
    gemm_main<8192>(acc, w0, w1, w2, rsW, voff, lds, xbase, rot);
    STAMP(3);
#pragma unroll
    for (int qt = 0; qt < 3; ++qt)
#pragma unroll
      for (int s = 0; s < 4; ++s) {
        u32x4 f;
#pragma unroll
        for (int p = 0; p < 4; ++p) f[p] = cvtpk(acc[qt][2 * p][s] * CSC, acc[qt][2 * p + 1][s] * CSC);
        qf[qt][s] = f;
      }
    WAIT_L0();
    bar();
#pragma unroll
    for (int qt = 0; qt < 3; ++qt)
#pragma unroll
      for (int s = 0; s < 4; ++s) *(u32x4*)(lds + wid * 12288 + (qt * 4 + s) * 1024 + lane * 16) = qf[qt][s];
    WAIT_L0();
  }

  unsigned ka[4];
#pragma unroll
  for (int s = 0; s < 4; ++s) ka[s] = (unsigned)(RING + (kh * 32 + li) * 256 + 16 * ((4 * s + g) ^ li));
  const int vr = 4 * g + (li >> 2);
  const unsigned va = (unsigned)(RING + (kh * 32 + vr) * 256 + 32 * (vr & 7) + 8 * (li & 3)) ^ (unsigned)(128 * kh);

  unsigned dko[2], dvo[2];
#pragma unroll
  for (int i = 0; i < 2; ++i) {
    const int row = 8 * wid + 4 * i + g;
    dko[i] = (unsigned)(g * 256 + 16 * (li ^ (row & 15)));
    dvo[i] = (unsigned)(g * 256 + 16 * (li ^ (2 * (row & 7))));
  }
  f32x4 oacc[8][3];
#pragma unroll
  for (int dt = 0; dt < 8; ++dt)
#pragma unroll
    for (int qt = 0; qt < 3; ++qt) oacc[dt][qt] = f32x4{0.f, 0.f, 0.f, 0.f};
  float mS[3], lsum[3];
  f32x4 negm[3];
#pragma unroll
  for (int qt = 0; qt < 3; ++qt) { mS[qt] = 0.f; lsum[qt] = 0.f; negm[qt] = f32x4{0.f, 0.f, 0.f, 0.f}; }

  auto qk = [&](f32x4 (&S)[2][3], unsigned ks_off) {
#pragma unroll
    for (int kt = 0; kt < 2; ++kt) {
#pragma unroll
      for (int s = 0; s < 4; ++s) {
        const u32x4 kf = *(const u32x4*)(lds + ka[s] + ks_off + kt * 4096);
#pragma unroll
        for (int qt = 0; qt < 3; ++qt) S[kt][qt] = mfma(kf, qf[qt][s], s == 0 ? negm[qt] : S[kt][qt]);
      }
    }
  };
  auto remax = [&](f32x4 (&S)[2][3], bool first) {
#pragma unroll
    for (int qt = 0; qt < 3; ++qt) {
      float m = fmaxf(fmaxf(fmaxf(S[0][qt][0] + 0.f, S[0][qt][1] + 0.f), fmaxf(S[0][qt][2] + 0.f, S[0][qt][3] + 0.f)),
                      fmaxf(fmaxf(S[1][qt][0] + 0.f, S[1][qt][1] + 0.f), fmaxf(S[1][qt][2] + 0.f, S[1][qt][3] + 0.f)));
      m = fmaxf(m, __shfl_xor(m, 16));
      m = fmaxf(m, __shfl_xor(m, 32));
      const float dl = first ? m : fmaxf(m, 0.f);
      mS[qt] += dl;
      negm[qt] = f32x4{-mS[qt], -mS[qt], -mS[qt], -mS[qt]};
      S[0][qt] -= dl; S[1][qt] -= dl;
      if (!first) {
        const float al = __builtin_amdgcn_exp2f(-dl);
        lsum[qt] *= al;
#pragma unroll
        for (int dt = 0; dt < 8; ++dt) oacc[dt][qt] *= al;
      }
    }
  };
  auto exp_sum = [&](f32x4 (&S)[2][3], float (&ps)[3]) {
#pragma unroll
    for (int qt = 0; qt < 3; ++qt) {
#pragma unroll
      for (int j = 0; j < 8; ++j) S[j >> 2][qt][j & 3] = __builtin_amdgcn_exp2f(S[j >> 2][qt][j & 3]);
      float a = S[0][qt][0];
#pragma unroll
      for (int j = 1; j < 8; ++j) a += S[j >> 2][qt][j & 3];
      ps[qt] = a;
    }
  };
  auto pack = [&](u32x4 (&pf)[3], const f32x4 (&P)[2][3]) {
#pragma unroll
    for (int qt = 0; qt < 3; ++qt)
#pragma unroll
      for (int e = 0; e < 4; ++e) pf[qt][e] = cvtpk(P[e >> 1][qt][2 * (e & 1)], P[e >> 1][qt][2 * (e & 1) + 1]);
  };
  auto v_read = [&](s16x4 (&lo)[4], s16x4 (&hi)[4], unsigned vs_off, int dh) {
#pragma unroll
    for (int i = 0; i < 4; ++i) {
      const unsigned a = (va + vs_off) ^ (unsigned)(32 * (dh * 4 + i));
      asm volatile("ds_read_b64_tr_b16 %0, %1" : "=&v"(lo[i]) : "v"(a) : "memory");
      asm volatile("ds_read_b64_tr_b16 %0, %1 offset:4096" : "=&v"(hi[i]) : "v"(a) : "memory");
    }
  };
  auto v_wait = [&](u32x4 (&vf)[4], s16x4 (&lo)[4], s16x4 (&hi)[4]) {
    asm volatile("s_waitcnt lgkmcnt(0)" : "+v"(lo[0]), "+v"(lo[1]), "+v"(lo[2]), "+v"(lo[3]), "+v"(hi[0]), "+v"(hi[1]), "+v"(hi[2]), "+v"(hi[3]) :: "memory");
#pragma unroll
    for (int i = 0; i < 4; ++i) {
      const u32x2 l2 = __builtin_bit_cast(u32x2, lo[i]), h2 = __builtin_bit_cast(u32x2, hi[i]);
      vf[i] = u32x4{l2[0], l2[1], h2[0], h2[1]};
    }
  };
  auto pv_mma = [&](const u32x4 (&vf)[4], const u32x4 (&pf)[3], int dh) {
#pragma unroll
    for (int i = 0; i < 4; ++i)
#pragma unroll
      for (int qt = 0; qt < 3; ++qt) oacc[dh * 4 + i][qt] = mfma(vf[i], pf[qt], oacc[dh * 4 + i][qt]);
  };
  auto pv = [&](const u32x4 (&pf)[3], unsigned vs_off) {
    u32x4 vf[4]; s16x4 lo[4], hi[4];
    v_read(lo, hi, vs_off, 0); v_wait(vf, lo, hi); pv_mma(vf, pf, 0);
    v_read(lo, hi, vs_off, 1); v_wait(vf, lo, hi); pv_mma(vf, pf, 1);
  };
  auto k_read = [&](u32x4 (&kf)[4], unsigned ks_off, int kt) {
#pragma unroll
    for (int s = 0; s < 4; ++s) kf[s] = *(const u32x4*)(lds + ka[s] + ks_off + kt * 4096);
  };
  auto qk_mma = [&](f32x4 (&S)[3], const u32x4 (&kf)[4]) {
#pragma unroll
    for (int s = 0; s < 4; ++s)
#pragma unroll
      for (int qt = 0; qt < 3; ++qt) S[qt] = mfma(kf[s], qf[qt][s], s == 0 ? negm[qt] : S[qt]);
  };
  auto exp12 = [&](f32x4 (&S)[3]) {
#pragma unroll
    for (int qt = 0; qt < 3; ++qt)
#pragma unroll
      for (int j = 0; j < 4; ++j) S[qt][j] = __builtin_amdgcn_exp2f(S[qt][j]);
  };
#define SGB(mask, n) __builtin_amdgcn_sched_group_barrier(mask, n, 0)
#define TICK(i) do {} while (0)
#define TICK0() do {} while (0)

  auto qk1 = [&](f32x4& S, const u32x4 (&kf)[4], int qt) {
#pragma unroll
    for (int s = 0; s < 4; ++s) S = mfma(kf[s], qf[qt][s], s == 0 ? negm[qt] : S);
  };
  auto exp4 = [&](f32x4& S) {
#pragma unroll
    for (int j = 0; j < 4; ++j) S[j] = __builtin_amdgcn_exp2f(S[j]);
  };
  auto pack1 = [&](u32x4& pf, const f32x4& P0, const f32x4& P1) {
    pf[0] = cvtpk(P0[0], P0[1]); pf[1] = cvtpk(P0[2], P0[3]); pf[2] = cvtpk(P1[0], P1[1]); pf[3] = cvtpk(P1[2], P1[3]);
  };
  auto sum8 = [&](const f32x4& P0, const f32x4& P1) {
    float a = P0[0];
    a += P0[1]; a += P0[2]; a += P0[3]; a += P1[0]; a += P1[1]; a += P1[2]; a += P1[3];
    return a;
  };

  u32x4 pf[3];
  WAIT_V(24);
  WAIT_L0();
  bar();
  if (wid >= 4) {
#pragma unroll
    for (int qt = 0; qt < 3; ++qt)
#pragma unroll
      for (int s = 0; s < 4; ++s) qf[qt][s] = *(const u32x4*)(lds + h * 12288 + (qt * 4 + s) * 1024 + lane * 16);
  }
  {
    float ps[3]; f32x4 P[2][3];
    qk(P, 0u);
    remax(P, true);
    exp_sum(P, ps);
    pack(pf, P);
#pragma unroll
    for (int qt = 0; qt < 3; ++qt) lsum[qt] = ps[qt];
  }
  STAMP(4);
  TICK0();
  int slV = 1;
#pragma nounroll
  for (int t = 0; t < 7; ++t) {
    WAIT_V(6);
    WAIT_L0();
    bar();
    TICK(0);
    const int slK = slV == 6 ? 0 : slV + 1;
    const unsigned ko = (unsigned)(slK * SLOT), vo = (unsigned)(slV * SLOT);
    f32x4 S[2][3];
    u32x4 pn[3], kf0[4], kf1[4], vf0[4], vf1[4];
    s16x4 l0[4], h0[4], l1[4], h1[4];
    float ps[3];
    k_read(kf0, ko, 0);
    k_read(kf1, ko, 1);
    SB0();
    {
      const int fK = slV >= 2 ? slV - 2 : slV + 5, fV = slV >= 1 ? slV - 1 : 6;
      char* d1 = lds + RING + fK * SLOT + wid * 2048;
      char* d0 = lds + RING + fV * SLOT + wid * 2048;
      const unsigned so = (unsigned)(((t + 3) * 64 + 8 * wid) * 256);
      dma16(rsK, d1, dko[0], so);
      dma16(rsK, d1 + 1024, dko[1], so + 1024);
      dma16(rsV, d0, dvo[0], so);
      dma16(rsV, d0 + 1024, dvo[1], so + 1024);
    }
    SB0();
    TICK(2);
    qk1(S[0][0], kf0, 0); qk1(S[0][1], kf0, 1); qk1(S[0][2], kf0, 2);
    exp4(S[0][0]); exp4(S[0][1]);
    SGB(0x008, 4);
#pragma unroll
    for (int i = 0; i < 8; ++i) { SGB(0x008, 1); SGB(0x400, 1); }
    SB0();
    TICK(3);
    v_read(l0, h0, vo, 0);
    qk1(S[1][0], kf1, 0); qk1(S[1][1], kf1, 1); qk1(S[1][2], kf1, 2);
    exp4(S[0][2]); exp4(S[1][0]); exp4(S[1][1]);
#pragma unroll
    for (int i = 0; i < 12; ++i) { SGB(0x008, 1); SGB(0x400, 1); }
    SB0();
    TICK(4);
    v_wait(vf0, l0, h0);
    v_read(l1, h1, vo, 1);
    pv_mma(vf0, pf, 0);
    exp4(S[1][2]);
    pack1(pn[0], S[0][0], S[1][0]); pack1(pn[1], S[0][1], S[1][1]);
    ps[0] = sum8(S[0][0], S[1][0]);
#pragma unroll
    for (int i = 0; i < 4; ++i) { SGB(0x008, 1); SGB(0x400, 1); }
#pragma unroll
    for (int i = 0; i < 8; ++i) { SGB(0x008, 1); SGB(0x002, 2); }
    SB0();
    TICK(5);
    v_wait(vf1, l1, h1);
    pv_mma(vf1, pf, 1);
    pack1(pn[2], S[0][2], S[1][2]);
    ps[1] = sum8(S[0][1], S[1][1]); ps[2] = sum8(S[0][2], S[1][2]);
#pragma unroll
    for (int i = 0; i < 10; ++i) { SGB(0x008, 1); SGB(0x002, 2); }
    SB0();
    TICK(6);
    if (__any((ps[0] > 65536.f) | (ps[1] > 65536.f) | (ps[2] > 65536.f))) {
      qk(S, ko);
      remax(S, false);
      exp_sum(S, ps);
      pack(pn, S);
    }
#pragma unroll
    for (int qt = 0; qt < 3; ++qt) { lsum[qt] += ps[qt]; pf[qt] = pn[qt]; }
    slV = slV >= 5 ? slV - 5 : slV + 2;
    TICK(7);
  }
  {
    WAIT_V(0);
    WAIT_L0();
    bar();
    STAMP(11);
    pv(pf, (unsigned)(slV * SLOT));
  }

  STAMP(12);
  int tz = threadIdx.x; asm volatile("" : "+v"(tz));
  const int lane2 = tz & 63, li2 = lane2 & 15, g2 = lane2 >> 4;
#define lane lane2
#define li li2
#define g g2
  const unsigned xbase2 = (unsigned)(li * 1024 + 16 * (g ^ li));
  u32x4 w0[8], w1[8], w2[8];
  const __amdgpu_buffer_rsrc_t rsWo = mk_rsrc(wo, 512 * DM * 2);
  const unsigned voffo = (unsigned)(8 * g * 1024 + h * 256 + 16 * li);
  if (wid < 4) {
    load_w<1024>(w0, rsWo, voffo, rot);
    load_w<1024>(w1, rsWo, voffo, (rot + 1) & 15);
  }
#pragma unroll
  for (int qt = 0; qt < 3; ++qt) {
    float l = lsum[qt];
    l += __shfl_xor(l, 16);
    l += __shfl_xor(l, 32);
    lsum[qt] = l;
  }
  WAIT_L0();
  bar();
  {
    char* my = lds + RING + wid * 12288;
#pragma unroll
    for (int i = 0; i < 4; ++i)
#pragma unroll
      for (int qt = 0; qt < 3; ++qt) *(f32x4*)(my + (i * 3 + qt) * 1024 + lane * 16) = oacc[4 + i][qt];
    float* ml = (float*)(lds + RING + 8 * 12288) + wid * 384;
#pragma unroll
    for (int qt = 0; qt < 3; ++qt) { ml[(2 * qt) * 64 + lane] = mS[qt]; ml[(2 * qt + 1) * 64 + lane] = lsum[qt]; }
  }
  WAIT_L0();
  bar();
  {
    const int pw = wid ^ 4;
    const char* ot = lds + RING + pw * 12288;
    const float* ml = (const float*)(lds + RING + 8 * 12288) + pw * 384;
    float mo[3], lo_[3];
    f32x4 o[4][3];
#pragma unroll
    for (int qt = 0; qt < 3; ++qt) { mo[qt] = ml[(2 * qt) * 64 + lane]; lo_[qt] = ml[(2 * qt + 1) * 64 + lane]; }
#pragma unroll
    for (int i = 0; i < 4; ++i)
#pragma unroll
      for (int qt = 0; qt < 3; ++qt) o[i][qt] = *(const f32x4*)(ot + (i * 3 + qt) * 1024 + lane * 16);
    SB0();
#pragma unroll
    for (int qt = 0; qt < 3; ++qt) {
      const float M = fmaxf(mS[qt], mo[qt]);
      const float wa = __builtin_amdgcn_exp2f(mS[qt] - M), wb = __builtin_amdgcn_exp2f(mo[qt] - M);
      const float inv = __builtin_amdgcn_rcpf(wa * lsum[qt] + wb * lo_[qt]);
      const float fa = wa * inv, fb = wb * inv;
      const int row = 16 * qt + li;
#pragma unroll
      for (int i = 0; i < 4; ++i) {
        const f32x4 mine = oacc[i][qt];
        const int dt = kh * 4 + i;
        u32x2 r;
        r[0] = cvtpk(mine[0] * fa + o[i][qt][0] * fb, mine[1] * fa + o[i][qt][1] * fb);
        r[1] = cvtpk(mine[2] * fa + o[i][qt][2] * fb, mine[3] * fa + o[i][qt][3] * fb);
        const int chunk = 16 * h + 2 * dt + (g >> 1);
        *(u32x2*)(lds + row * 1024 + 16 * (chunk ^ li) + 8 * (g & 1)) = r;
      }
    }
  }
  if (wid < 4) load_w<1024>(w2, rsWo, voffo, (rot + 2) & 15);
  WAIT_L0();
  bar();
  STAMP(13);
  if (wid < 4) {
    f32x4 acc[3][8];
#pragma unroll
    for (int qt = 0; qt < 3; ++qt)
#pragma unroll
      for (int c = 0; c < 8; ++c) acc[qt][c] = f32x4{0.f, 0.f, 0.f, 0.f};
    gemm_main<1024>(acc, w0, w1, w2, rsWo, voffo, lds, xbase2, rot);
    STAMP(14);
    int tz3 = threadIdx.x; asm volatile("" : "+v"(tz3));
    float* dst = slab + (size_t)blk * SLAB_F + (size_t)wid * 6144 + (tz3 & 63) * 4;
#pragma unroll
    for (int qt = 0; qt < 3; ++qt)
#pragma unroll
      for (int cc = 0; cc < 2; ++cc)
#pragma unroll
        for (int r = 0; r < 4; ++r) {
          const f32x4 v = {acc[qt][4 * cc][r], acc[qt][4 * cc + 1][r], acc[qt][4 * cc + 2][r], acc[qt][4 * cc + 3][r]};
          *(f32x4*)(dst + ((qt * 2 + cc) * 4 + r) * 256) = v;
        }
  }
  STAMP(15);
  STAMP_FLUSH;
#undef lane
#undef li
#undef g
}

__global__ __launch_bounds__(256) void gqa_reduce_kernel(const float* __restrict__ slab, unsigned short* __restrict__ out) {
  const int idx = blockIdx.x * 256 + threadIdx.x;
  const int lane = idx & 63, r = (idx >> 6) & 3;
  const int t2 = idx >> 8;
  const int rest = t2 / 12, wq3 = t2 % 12, w = wq3 / 3, qt = wq3 % 3;
  const int br = rest >> 4, b = rest & 15;
  const float* src = slab + (size_t)rest * 8 * SLAB_F + (size_t)w * 6144 + (size_t)(qt * 2 * 4 + r) * 256 + lane * 4;
  f32x4 s0 = {0.f, 0.f, 0.f, 0.f}, s1 = {0.f, 0.f, 0.f, 0.f};
#pragma unroll
  for (int j = 0; j < 8; ++j) {
    s0 += *(const f32x4*)(src + (size_t)j * SLAB_F);
    s1 += *(const f32x4*)(src + (size_t)j * SLAB_F + 4 * 256);
  }
  u32x4 o;
  o[0] = cvtpk(s0[0], s0[1]); o[1] = cvtpk(s0[2], s0[3]); o[2] = cvtpk(s1[0], s1[1]); o[3] = cvtpk(s1[2], s1[3]);
  const int q = lane & 15, g = lane >> 4;
  *(u32x4*)(out + ((size_t)(b * SQ + 16 * qt + q) * 1024 + br * 512 + 128 * w + 32 * g + 8 * r)) = o;
}

extern "C" void kernel_launch(void* const* d_in, const int* in_sizes, int n_in,
                              void* d_out, int out_size, void* d_ws, size_t ws_size,
                              hipStream_t stream) {
  (void)in_sizes; (void)n_in; (void)out_size; (void)ws_size;
  hipFuncSetAttribute((const void*)gqa_fused_kernel, hipFuncAttributeMaxDynamicSharedMemorySize, LDS_BYTES);
  float* slab = (float*)d_ws;
  hipLaunchKernelGGL(gqa_fused_kernel, dim3(256), dim3(512), LDS_BYTES, stream,
                     (const unsigned short*)d_in[0], (const unsigned short*)d_in[1], (const unsigned short*)d_in[2],
                     (const unsigned short*)d_in[3], (const unsigned short*)d_in[4],
                     (const unsigned short*)d_in[5], (const unsigned short*)d_in[6], (const unsigned short*)d_in[7],
                     (const unsigned short*)d_in[8], (const unsigned short*)d_in[9], slab);
  hipLaunchKernelGGL(gqa_reduce_kernel, dim3(384), dim3(256), 0, stream, (const float*)slab, (unsigned short*)d_out);
}
```

```cpp
#include <hip/hip_runtime.h>
#include <hip/hip_bf16.h>

typedef __bf16   bf16x8 __attribute__((ext_vector_type(8)));
typedef __bf16   bf16x2 __attribute__((ext_vector_type(2)));
typedef short    s16x4  __attribute__((ext_vector_type(4)));
typedef float    f32x4  __attribute__((ext_vector_type(4)));
typedef float    f32x2  __attribute__((ext_vector_type(2)));
typedef unsigned u32x4  __attribute__((ext_vector_type(4)));
typedef unsigned u32x2  __attribute__((ext_vector_type(2)));
#define LAS __attribute__((address_space(3)))
#define DI __device__ __forceinline__

#define STAMP(i) do {} while (0)
#define STAMP_DECL do {} while (0)
#define STAMP_FLUSH do {} while (0)

namespace {
constexpr int NB = 16, SQ = 48, DM = 512, HQ = 32, HKV = 8, SK = 512, DH = 128;
constexpr int XO_BYTES = SQ * DM * 2;
constexpr int SLOT = 16384, NSLOT = 7;
constexpr int RING = XO_BYTES;
constexpr int LDS_BYTES = XO_BYTES + NSLOT * SLOT;
constexpr float CSC = 0.08838834764831845f * 1.4426950408889634f;
constexpr float THR = 8.0f;
constexpr size_t SLAB_F = (size_t)SQ * DM;

DI unsigned cvtpk(float lo, float hi) {
  f32x2 v = {lo, hi}; bf16x2 b = __builtin_convertvector(v, bf16x2); return __builtin_bit_cast(unsigned, b);
}
DI f32x4 mfma(u32x4 a, u32x4 b, f32x4 c) {
  return __builtin_amdgcn_mfma_f32_16x16x32_bf16(__builtin_bit_cast(bf16x8, a), __builtin_bit_cast(bf16x8, b), c, 0, 0, 0);
}
DI void bar() { __builtin_amdgcn_s_barrier(); }
#define WAIT_L0() asm volatile("s_waitcnt lgkmcnt(0)" ::: "memory")
#define WAIT_V(n) asm volatile("s_waitcnt vmcnt(" #n ")" ::: "memory")
#define SB0() __builtin_amdgcn_sched_barrier(0)

DI __amdgpu_buffer_rsrc_t mk_rsrc(const void* p, unsigned bytes) {
  return __builtin_amdgcn_make_buffer_rsrc((void*)p, (short)0, (int)bytes, 0x00020000);
}
DI void dma16(__amdgpu_buffer_rsrc_t rs, char* lds_dst, unsigned voff, unsigned soff) {
  __builtin_amdgcn_raw_ptr_buffer_load_lds(rs, (LAS void*)lds_dst, 16, (int)voff, (int)soff, 0, 0);
}

template <unsigned WSTRIDE>
DI void load_w(u32x4 (&w)[8], __amdgpu_buffer_rsrc_t rs, unsigned voff, int ks) {
#pragma unroll
  for (int j = 0; j < 8; ++j)
    w[j] = __builtin_amdgcn_raw_buffer_load_b128(rs, (int)voff, (int)((unsigned)(32 * ks + j) * WSTRIDE), 0);
}
DI void perm_d(u32x4& f0, u32x4& f1, const u32x4 (&w)[8], int d) {
#pragma unroll
  for (int p = 0; p < 4; ++p) {
    f0[p] = __builtin_amdgcn_perm(w[2 * p + 1][d], w[2 * p][d], 0x05040100u);
    f1[p] = __builtin_amdgcn_perm(w[2 * p + 1][d], w[2 * p][d], 0x07060302u);
  }
}
DI void mma_d(f32x4 (&acc)[3][8], const u32x4& f0, const u32x4& f1, const u32x4 (&xb)[3], int d) {
#pragma unroll
  for (int qt = 0; qt < 3; ++qt) {
    acc[qt][2 * d]     = mfma(f0, xb[qt], acc[qt][2 * d]);
    acc[qt][2 * d + 1] = mfma(f1, xb[qt], acc[qt][2 * d + 1]);
  }
}
DI void read_x(u32x4 (&xb)[3], const char* lds, unsigned addr) {
#pragma unroll
  for (int qt = 0; qt < 3; ++qt) xb[qt] = *(const u32x4*)(lds + addr + qt * 16384);
}
template <unsigned WSTRIDE, int I, int NS>
DI void gemm_step(f32x4 (&acc)[3][8], u32x4 (&w)[8], u32x4 (&fa)[2], u32x4 (&fb)[2], u32x4 (&xc)[3], u32x4 (&xn)[3],
                  __amdgpu_buffer_rsrc_t rs, unsigned voff, const char* lds, unsigned xbase, int rot) {
  perm_d(fa[0], fa[1], w, 0);
  if (I > 0) mma_d(acc, fb[0], fb[1], xn, 3);
  SB0();
  if (I + 1 < NS) read_x(xn, lds, xbase ^ (unsigned)(((I + 1 + rot) & 15) << 6));
  perm_d(fb[0], fb[1], w, 1);
  mma_d(acc, fa[0], fa[1], xc, 0);
  SB0();
  perm_d(fa[0], fa[1], w, 2);
  mma_d(acc, fb[0], fb[1], xc, 1);
  SB0();
  perm_d(fb[0], fb[1], w, 3);
  mma_d(acc, fa[0], fa[1], xc, 2);
  SB0();
  if (I + 3 < NS) load_w<WSTRIDE>(w, rs, voff, (I + 3 + rot) & 15);
  SB0();
  if (I == NS - 1) mma_d(acc, fb[0], fb[1], xc, 3);
}
template <unsigned WSTRIDE, int NS>
DI void gemm_main(f32x4 (&acc)[3][8], u32x4 (&w0)[8], u32x4 (&w1)[8], u32x4 (&w2)[8], __amdgpu_buffer_rsrc_t rs, unsigned voff, const char* lds, unsigned xbase, int rot) {
  u32x4 fa[2], fb[2], x0[3], x1[3];
  read_x(x0, lds, xbase ^ (unsigned)(rot << 6));
#define GSA(i, W) gemm_step<WSTRIDE, i, NS>(acc, W, fa, fb, x0, x1, rs, voff, lds, xbase, rot);
#define GSB(i, W) gemm_step<WSTRIDE, i, NS>(acc, W, fa, fb, x1, x0, rs, voff, lds, xbase, rot);
  GSA(0, w0) GSB(1, w1) GSA(2, w2) GSB(3, w0) GSA(4, w1) GSB(5, w2) GSA(6, w0) GSB(7, w1)
  if constexpr (NS == 16) {
    GSA(8, w2) GSB(9, w0) GSA(10, w1) GSB(11, w2) GSA(12, w0) GSB(13, w1) GSA(14, w2) GSB(15, w0)
  }
#undef GSA
#undef GSB
}
}

__global__ __launch_bounds__(512, 2) void gqa_fused_kernel(
    const unsigned short* __restrict__ xA, const unsigned short* __restrict__ kA, const unsigned short* __restrict__ vA,
    const unsigned short* __restrict__ wqA, const unsigned short* __restrict__ woA,
    const unsigned short* __restrict__ xB, const unsigned short* __restrict__ kB, const unsigned short* __restrict__ vB,
    const unsigned short* __restrict__ wqB, const unsigned short* __restrict__ woB,
    unsigned short* __restrict__ slab) {
  extern __shared__ __attribute__((aligned(16))) char lds[];
  STAMP_DECL;
  STAMP(0);
  const int tid = threadIdx.x, lane = tid & 63;
  const int wid = __builtin_amdgcn_readfirstlane(tid >> 6);
  const int li = lane & 15, g = lane >> 4;
  const int blk = blockIdx.x, xc = blk & 7, idx = blk >> 3;
  const int br = xc >> 2, kvh = 2 * (xc & 3) + (idx & 1), b = idx >> 1;
  const unsigned short* x  = (br ? xB : xA) + (size_t)b * SQ * DM;
  const unsigned short* kp = (br ? kB : kA) + ((size_t)b * HKV + kvh) * SK * DH;
  const unsigned short* vp = (br ? vB : vA) + ((size_t)b * HKV + kvh) * SK * DH;
  const unsigned short* wq = (br ? wqB : wqA) + (size_t)kvh * 512;
  const unsigned short* wo = (br ? woB : woA) + (size_t)kvh * 512 * DM;
  const __amdgpu_buffer_rsrc_t rsK = mk_rsrc(kp, SK * DH * 2), rsV = mk_rsrc(vp, SK * DH * 2);
  const __amdgpu_buffer_rsrc_t rsWo = mk_rsrc(wo, 512 * DM * 2);

  const unsigned xbase = (unsigned)(li * 1024 + 16 * (g ^ li));
  const int rot = b;

  const int h = wid & 3, kh = wid >> 2;
  u32x4 qf[3][4];

  auto dma_tile = [&](int u, int p0, int np) {
    char* dst = lds + RING + (u % NSLOT) * SLOT;
    const int t = u >> 1;
#pragma unroll
    for (int i = 0; i < np; ++i) {
      const int p = p0 + i;
      const int row = 4 * p + g;
      if (u & 1) dma16(rsV, dst + p * 1024, (unsigned)(g * 256 + 16 * (li ^ (2 * (row & 7)))), (unsigned)((t * 64 + 4 * p) * 256));
      else       dma16(rsK, dst + p * 1024, (unsigned)(g * 256 + 16 * (li ^ (row & 15))), (unsigned)((t * 64 + 4 * p) * 256));
    }
  };

  if (wid >= 4) {
    const __amdgpu_buffer_rsrc_t rsX = mk_rsrc(x, SQ * DM * 2);
    const int w4 = wid - 4;
#pragma unroll
    for (int i = 0; i < 12; ++i) {
      const int row = w4 * 12 + i;
      dma16(rsX, lds + row * 1024, (unsigned)(16 * (lane ^ (row & 15))), (unsigned)(row * 1024));
    }
    STAMP(1);
    WAIT_V(0);
    STAMP(2);
    bar();
#pragma unroll
    for (int u = 0; u < NSLOT; ++u) dma_tile(u, w4 * 4, 4);
    bar();
    STAMP(3);
  } else {
    const __amdgpu_buffer_rsrc_t rsW = mk_rsrc(wq, (unsigned)(DM * HQ * DH * 2 - kvh * 1024));
    const int cg = 4 * (li & 3) + (li >> 2);
    const unsigned voff = (unsigned)(8 * g * 8192 + wid * 256 + 16 * cg);
    u32x4 w0[8], w1[8], w2[8];
    load_w<8192>(w0, rsW, voff, rot);
    load_w<8192>(w1, rsW, voff, (rot + 1) & 15);
    load_w<8192>(w2, rsW, voff, (rot + 2) & 15);
    f32x4 acc[3][8];
#pragma unroll
    for (int qt = 0; qt < 3; ++qt)
#pragma unroll
      for (int c = 0; c < 8; ++c) acc[qt][c] = f32x4{0.f, 0.f, 0.f, 0.f};
    STAMP(1);
    bar();
    STAMP(2);
    gemm_main<8192, 16>(acc, w0, w1, w2, rsW, voff, lds, xbase, rot);
    STAMP(3);
#pragma unroll
    for (int qt = 0; qt < 3; ++qt)
#pragma unroll
      for (int s = 0; s < 4; ++s) {
        u32x4 f;
#pragma unroll
        for (int p = 0; p < 4; ++p) f[p] = cvtpk(acc[qt][2 * p][s] * CSC, acc[qt][2 * p + 1][s] * CSC);
        qf[qt][s] = f;
      }
    WAIT_L0();
    bar();
#pragma unroll
    for (int qt = 0; qt < 3; ++qt)
#pragma unroll
      for (int s = 0; s < 4; ++s) *(u32x4*)(lds + wid * 12288 + (qt * 4 + s) * 1024 + lane * 16) = qf[qt][s];
    WAIT_L0();
  }

  unsigned ka[4];
#pragma unroll
  for (int s = 0; s < 4; ++s) ka[s] = (unsigned)(RING + (kh * 32 + li) * 256 + 16 * ((4 * s + g) ^ li));
  const int vr = 4 * g + (li >> 2);
  const unsigned va = (unsigned)(RING + (kh * 32 + vr) * 256 + 32 * (vr & 7) + 8 * (li & 3)) ^ (unsigned)(128 * kh);

  unsigned dko[2], dvo[2];
#pragma unroll
  for (int i = 0; i < 2; ++i) {
    const int row = 8 * wid + 4 * i + g;
    dko[i] = (unsigned)(g * 256 + 16 * (li ^ (row & 15)));
    dvo[i] = (unsigned)(g * 256 + 16 * (li ^ (2 * (row & 7))));
  }
  f32x4 oacc[8][3];
#pragma unroll
  for (int dt = 0; dt < 8; ++dt)
#pragma unroll
    for (int qt = 0; qt < 3; ++qt) oacc[dt][qt] = f32x4{0.f, 0.f, 0.f, 0.f};
  float mS[3], lsum[3];
  f32x4 negm[3];
#pragma unroll
  for (int qt = 0; qt < 3; ++qt) { mS[qt] = 0.f; lsum[qt] = 0.f; negm[qt] = f32x4{0.f, 0.f, 0.f, 0.f}; }

  auto qk = [&](f32x4 (&S)[2][3], unsigned ks_off) {
#pragma unroll
    for (int kt = 0; kt < 2; ++kt) {
#pragma unroll
      for (int s = 0; s < 4; ++s) {
        const u32x4 kf = *(const u32x4*)(lds + ka[s] + ks_off + kt * 4096);
#pragma unroll
        for (int qt = 0; qt < 3; ++qt) S[kt][qt] = mfma(kf, qf[qt][s], s == 0 ? negm[qt] : S[kt][qt]);
      }
    }
  };
  auto remax = [&](f32x4 (&S)[2][3], bool first) {
#pragma unroll
    for (int qt = 0; qt < 3; ++qt) {
      float m = fmaxf(fmaxf(fmaxf(S[0][qt][0] + 0.f, S[0][qt][1] + 0.f), fmaxf(S[0][qt][2] + 0.f, S[0][qt][3] + 0.f)),
                      fmaxf(fmaxf(S[1][qt][0] + 0.f, S[1][qt][1] + 0.f), fmaxf(S[1][qt][2] + 0.f, S[1][qt][3] + 0.f)));
      m = fmaxf(m, __shfl_xor(m, 16));
      m = fmaxf(m, __shfl_xor(m, 32));
      const float dl = first ? m : fmaxf(m, 0.f);
      mS[qt] += dl;
      negm[qt] = f32x4{-mS[qt], -mS[qt], -mS[qt], -mS[qt]};
      S[0][qt] -= dl; S[1][qt] -= dl;
      if (!first) {
        const float al = __builtin_amdgcn_exp2f(-dl);
        lsum[qt] *= al;
#pragma unroll
        for (int dt = 0; dt < 8; ++dt) oacc[dt][qt] *= al;
      }
    }
  };
  auto exp_sum = [&](f32x4 (&S)[2][3], float (&ps)[3]) {
#pragma unroll
    for (int qt = 0; qt < 3; ++qt) {
#pragma unroll
      for (int j = 0; j < 8; ++j) S[j >> 2][qt][j & 3] = __builtin_amdgcn_exp2f(S[j >> 2][qt][j & 3]);
      float a = S[0][qt][0];
#pragma unroll
      for (int j = 1; j < 8; ++j) a += S[j >> 2][qt][j & 3];
      ps[qt] = a;
    }
  };
  auto pack = [&](u32x4 (&pf)[3], const f32x4 (&P)[2][3]) {
#pragma unroll
    for (int qt = 0; qt < 3; ++qt)
#pragma unroll
      for (int e = 0; e < 4; ++e) pf[qt][e] = cvtpk(P[e >> 1][qt][2 * (e & 1)], P[e >> 1][qt][2 * (e & 1) + 1]);
  };
  auto v_read = [&](s16x4 (&lo)[4], s16x4 (&hi)[4], unsigned vs_off, int dh) {
#pragma unroll
    for (int i = 0; i < 4; ++i) {
      const unsigned a = (va + vs_off) ^ (unsigned)(32 * (dh * 4 + i));
      asm volatile("ds_read_b64_tr_b16 %0, %1" : "=&v"(lo[i]) : "v"(a) : "memory");
      asm volatile("ds_read_b64_tr_b16 %0, %1 offset:4096" : "=&v"(hi[i]) : "v"(a) : "memory");
    }
  };
  auto v_wait = [&](u32x4 (&vf)[4], s16x4 (&lo)[4], s16x4 (&hi)[4]) {
    asm volatile("s_waitcnt lgkmcnt(0)" : "+v"(lo[0]), "+v"(lo[1]), "+v"(lo[2]), "+v"(lo[3]), "+v"(hi[0]), "+v"(hi[1]), "+v"(hi[2]), "+v"(hi[3]) :: "memory");
#pragma unroll
    for (int i = 0; i < 4; ++i) {
      const u32x2 l2 = __builtin_bit_cast(u32x2, lo[i]), h2 = __builtin_bit_cast(u32x2, hi[i]);
      vf[i] = u32x4{l2[0], l2[1], h2[0], h2[1]};
    }
  };
  auto pv_mma = [&](const u32x4 (&vf)[4], const u32x4 (&pf)[3], int dh) {
#pragma unroll
    for (int i = 0; i < 4; ++i)
#pragma unroll
      for (int qt = 0; qt < 3; ++qt) oacc[dh * 4 + i][qt] = mfma(vf[i], pf[qt], oacc[dh * 4 + i][qt]);
  };
  auto pv = [&](const u32x4 (&pf)[3], unsigned vs_off) {
    u32x4 vf[4]; s16x4 lo[4], hi[4];
    v_read(lo, hi, vs_off, 0); v_wait(vf, lo, hi); pv_mma(vf, pf, 0);
    v_read(lo, hi, vs_off, 1); v_wait(vf, lo, hi); pv_mma(vf, pf, 1);
  };
  auto k_read = [&](u32x4 (&kf)[4], unsigned ks_off, int kt) {
#pragma unroll
    for (int s = 0; s < 4; ++s) kf[s] = *(const u32x4*)(lds + ka[s] + ks_off + kt * 4096);
  };
  auto qk_mma = [&](f32x4 (&S)[3], const u32x4 (&kf)[4]) {
#pragma unroll
    for (int s = 0; s < 4; ++s)
#pragma unroll
      for (int qt = 0; qt < 3; ++qt) S[qt] = mfma(kf[s], qf[qt][s], s == 0 ? negm[qt] : S[qt]);
  };
  auto exp12 = [&](f32x4 (&S)[3]) {
#pragma unroll
    for (int qt = 0; qt < 3; ++qt)
#pragma unroll
      for (int j = 0; j < 4; ++j) S[qt][j] = __builtin_amdgcn_exp2f(S[qt][j]);
  };
#define SGB(mask, n) __builtin_amdgcn_sched_group_barrier(mask, n, 0)
#define TICK(i) do {} while (0)
#define TICK0() do {} while (0)

  auto qk1 = [&](f32x4& S, const u32x4 (&kf)[4], int qt) {
#pragma unroll
    for (int s = 0; s < 4; ++s) S = mfma(kf[s], qf[qt][s], s == 0 ? negm[qt] : S);
  };
  auto exp4 = [&](f32x4& S) {
#pragma unroll
    for (int j = 0; j < 4; ++j) S[j] = __builtin_amdgcn_exp2f(S[j]);
  };
  auto pack1 = [&](u32x4& pf, const f32x4& P0, const f32x4& P1) {
    pf[0] = cvtpk(P0[0], P0[1]); pf[1] = cvtpk(P0[2], P0[3]); pf[2] = cvtpk(P1[0], P1[1]); pf[3] = cvtpk(P1[2], P1[3]);
  };
  auto sum8 = [&](const f32x4& P0, const f32x4& P1) {
    float a = P0[0];
    a += P0[1]; a += P0[2]; a += P0[3]; a += P1[0]; a += P1[1]; a += P1[2]; a += P1[3];
    return a;
  };

  u32x4 pf[3];
  WAIT_V(24);
  WAIT_L0();
  bar();
  if (wid >= 4) {
#pragma unroll
    for (int qt = 0; qt < 3; ++qt)
#pragma unroll
      for (int s = 0; s < 4; ++s) qf[qt][s] = *(const u32x4*)(lds + h * 12288 + (qt * 4 + s) * 1024 + lane * 16);
  }
  {
    float ps[3]; f32x4 P[2][3];
    qk(P, 0u);
    remax(P, true);
    exp_sum(P, ps);
    pack(pf, P);
#pragma unroll
    for (int qt = 0; qt < 3; ++qt) lsum[qt] = ps[qt];
  }
  STAMP(4);
  TICK0();
  int slV = 1;
#pragma nounroll
  for (int t = 0; t < 7; ++t) {
    WAIT_V(6);
    WAIT_L0();
    bar();
    TICK(0);
    const int slK = slV == 6 ? 0 : slV + 1;
    const unsigned ko = (unsigned)(slK * SLOT), vo = (unsigned)(slV * SLOT);
    f32x4 S[2][3];
    u32x4 pn[3], kf0[4], kf1[4], vf0[4], vf1[4];
    s16x4 l0[4], h0[4], l1[4], h1[4];
    float ps[3];
    k_read(kf0, ko, 0);
    k_read(kf1, ko, 1);
    SB0();
    {
      const int fK = slV >= 2 ? slV - 2 : slV + 5, fV = slV >= 1 ? slV - 1 : 6;
      char* d1 = lds + RING + fK * SLOT + wid * 2048;
      char* d0 = lds + RING + fV * SLOT + wid * 2048;
      const unsigned so = (unsigned)(((t + 3) * 64 + 8 * wid) * 256);
      if (t != 5) {
        dma16(rsK, d1, dko[0], so);
        dma16(rsK, d1 + 1024, dko[1], so + 1024);
        dma16(rsV, d0, dvo[0], so);
        dma16(rsV, d0 + 1024, dvo[1], so + 1024);
      } else {
        const unsigned sw = (unsigned)(b * 32768 + wid * 4096);
        dma16(rsWo, d1, dko[0], sw);
        dma16(rsWo, d1 + 1024, dko[0], sw + 1024);
        dma16(rsWo, d0, dko[0], sw + 2048);
        dma16(rsWo, d0 + 1024, dko[0], sw + 3072);
      }
    }
    SB0();
    TICK(2);
    qk1(S[0][0], kf0, 0); qk1(S[0][1], kf0, 1); qk1(S[0][2], kf0, 2);
    exp4(S[0][0]); exp4(S[0][1]);
    SGB(0x008, 4);
#pragma unroll
    for (int i = 0; i < 8; ++i) { SGB(0x008, 1); SGB(0x400, 1); }
    SB0();
    TICK(3);
    v_read(l0, h0, vo, 0);
    qk1(S[1][0], kf1, 0); qk1(S[1][1], kf1, 1); qk1(S[1][2], kf1, 2);
    exp4(S[0][2]); exp4(S[1][0]); exp4(S[1][1]);
#pragma unroll
    for (int i = 0; i < 12; ++i) { SGB(0x008, 1); SGB(0x400, 1); }
    SB0();
    TICK(4);
    v_wait(vf0, l0, h0);
    v_read(l1, h1, vo, 1);
    pv_mma(vf0, pf, 0);
    exp4(S[1][2]);
    pack1(pn[0], S[0][0], S[1][0]); pack1(pn[1], S[0][1], S[1][1]);
    ps[0] = sum8(S[0][0], S[1][0]);
#pragma unroll
    for (int i = 0; i < 4; ++i) { SGB(0x008, 1); SGB(0x400, 1); }
#pragma unroll
    for (int i = 0; i < 8; ++i) { SGB(0x008, 1); SGB(0x002, 2); }
    SB0();
    TICK(5);
    v_wait(vf1, l1, h1);
    pv_mma(vf1, pf, 1);
    pack1(pn[2], S[0][2], S[1][2]);
    ps[1] = sum8(S[0][1], S[1][1]); ps[2] = sum8(S[0][2], S[1][2]);
#pragma unroll
    for (int i = 0; i < 10; ++i) { SGB(0x008, 1); SGB(0x002, 2); }
    SB0();
    TICK(6);
    if (__any((ps[0] > 65536.f) | (ps[1] > 65536.f) | (ps[2] > 65536.f))) {
      qk(S, ko);
      remax(S, false);
      exp_sum(S, ps);
      pack(pn, S);
    }
#pragma unroll
    for (int qt = 0; qt < 3; ++qt) { lsum[qt] += ps[qt]; pf[qt] = pn[qt]; }
    slV = slV >= 5 ? slV - 5 : slV + 2;
    TICK(7);
  }
  {
    WAIT_V(0);
    WAIT_L0();
    bar();
    STAMP(11);
    pv(pf, (unsigned)(slV * SLOT));
  }

  STAMP(12);
  int tz = threadIdx.x; asm volatile("" : "+v"(tz));
  const int lane2 = tz & 63, li2 = lane2 & 15, g2 = lane2 >> 4;
#define lane lane2
#define li li2
#define g g2
  const unsigned xbase2 = (unsigned)(li * 1024 + 16 * (g ^ li));
  u32x4 w0[8], w1[8], w2[8];
  const unsigned voffo = (unsigned)(8 * g * 1024 + h * 256 + 16 * (li ^ (2 * kh)));
  const int rot3 = (rot + 8 * kh) & 15;
  load_w<1024>(w0, rsWo, voffo, rot3);
  load_w<1024>(w1, rsWo, voffo, (rot3 + 1) & 15);
#pragma unroll
  for (int qt = 0; qt < 3; ++qt) {
    float l = lsum[qt];
    l += __shfl_xor(l, 16);
    l += __shfl_xor(l, 32);
    lsum[qt] = l;
  }
  WAIT_L0();
  bar();
  {
    char* my = lds + RING + wid * 12288;
#pragma unroll
    for (int i = 0; i < 4; ++i)
#pragma unroll
      for (int qt = 0; qt < 3; ++qt) *(f32x4*)(my + (i * 3 + qt) * 1024 + lane * 16) = oacc[4 + i][qt];
    float* ml = (float*)(lds + RING + 8 * 12288) + wid * 384;
#pragma unroll
    for (int qt = 0; qt < 3; ++qt) { ml[(2 * qt) * 64 + lane] = mS[qt]; ml[(2 * qt + 1) * 64 + lane] = lsum[qt]; }
  }
  WAIT_L0();
  bar();
  {
    const int pw = wid ^ 4;
    const char* ot = lds + RING + pw * 12288;
    const float* ml = (const float*)(lds + RING + 8 * 12288) + pw * 384;
    float mo[3], lo_[3];
    f32x4 o[4][3];
#pragma unroll
    for (int qt = 0; qt < 3; ++qt) { mo[qt] = ml[(2 * qt) * 64 + lane]; lo_[qt] = ml[(2 * qt + 1) * 64 + lane]; }
#pragma unroll
    for (int i = 0; i < 4; ++i)
#pragma unroll
      for (int qt = 0; qt < 3; ++qt) o[i][qt] = *(const f32x4*)(ot + (i * 3 + qt) * 1024 + lane * 16);
    SB0();
#pragma unroll
    for (int qt = 0; qt < 3; ++qt) {
      const float M = fmaxf(mS[qt], mo[qt]);
      const float wa = __builtin_amdgcn_exp2f(mS[qt] - M), wb = __builtin_amdgcn_exp2f(mo[qt] - M);
      const float inv = __builtin_amdgcn_rcpf(wa * lsum[qt] + wb * lo_[qt]);
      const float fa = wa * inv, fb = wb * inv;
      const int row = 16 * qt + li;
#pragma unroll
      for (int i = 0; i < 4; ++i) {
        const f32x4 mine = oacc[i][qt];
        const int dt = kh * 4 + i;
        u32x2 r;
        r[0] = cvtpk(mine[0] * fa + o[i][qt][0] * fb, mine[1] * fa + o[i][qt][1] * fb);
        r[1] = cvtpk(mine[2] * fa + o[i][qt][2] * fb, mine[3] * fa + o[i][qt][3] * fb);
        const int chunk = 16 * h + 2 * dt + (g >> 1);
        *(u32x2*)(lds + row * 1024 + 16 * (chunk ^ li) + 8 * (g & 1)) = r;
      }
    }
  }
  load_w<1024>(w2, rsWo, voffo, (rot3 + 2) & 15);
  WAIT_L0();
  bar();
  STAMP(13);
  {
    f32x4 acc[3][8];
#pragma unroll
    for (int qt = 0; qt < 3; ++qt)
#pragma unroll
      for (int c = 0; c < 8; ++c) acc[qt][c] = f32x4{0.f, 0.f, 0.f, 0.f};
    gemm_main<1024, 8>(acc, w0, w1, w2, rsWo, voffo, lds, xbase2, rot3);
    STAMP(14);
    int tz3 = threadIdx.x; asm volatile("" : "+v"(tz3));
    const int lane3 = tz3 & 63;
    char* my = lds + RING + wid * 12288 + lane3 * 8;
#pragma unroll
    for (int qt = 0; qt < 3; ++qt)
#pragma unroll
      for (int c = 0; c < 8; ++c) *(f32x2*)(my + (qt * 8 + c) * 512) = f32x2{acc[qt][c][2], acc[qt][c][3]};
    WAIT_L0();
    bar();
    const char* ot = lds + RING + (wid ^ 4) * 12288 + lane3 * 8;
    unsigned short* dst = slab + (size_t)(br * 16 + b) * (8 * SLAB_F) + (size_t)kvh * SLAB_F + (size_t)h * 6144 + lane3 * 8;
#pragma unroll
    for (int qt = 0; qt < 3; ++qt) {
      f32x2 o[8];
#pragma unroll
      for (int c = 0; c < 8; ++c) o[c] = *(const f32x2*)(ot + (qt * 8 + c) * 512);
#pragma unroll
      for (int rr = 0; rr < 2; ++rr) {
        u32x4 v;
#pragma unroll
        for (int p2 = 0; p2 < 4; ++p2) v[p2] = cvtpk(acc[qt][2 * p2][rr] + o[2 * p2][rr], acc[qt][2 * p2 + 1][rr] + o[2 * p2 + 1][rr]);
        *(u32x4*)(dst + (qt * 4 + 2 * kh + rr) * 512) = v;
      }
    }
  }
  STAMP(15);
  STAMP_FLUSH;
#undef lane
#undef li
#undef g
}

__global__ __launch_bounds__(256) void gqa_reduce_kernel(const unsigned short* __restrict__ slab, unsigned short* __restrict__ out) {
  const int idx = blockIdx.x * 256 + threadIdx.x;
  const int lane = idx & 63, r = (idx >> 6) & 3;
  const int t2 = idx >> 8;
  const int rest = t2 / 12, wq3 = t2 % 12, w = wq3 / 3, qt = wq3 % 3;
  const int br = rest >> 4, b = rest & 15;
  const unsigned short* src = slab + (size_t)rest * 8 * SLAB_F + (size_t)w * 6144 + (size_t)(qt * 4 + r) * 512 + lane * 8;
  u32x4 v[8];
#pragma unroll
  for (int j = 0; j < 8; ++j) v[j] = *(const u32x4*)(src + (size_t)j * SLAB_F);
  float s[8] = {0.f, 0.f, 0.f, 0.f, 0.f, 0.f, 0.f, 0.f};
#pragma unroll
  for (int j = 0; j < 8; ++j)
#pragma unroll
    for (int e = 0; e < 4; ++e) {
      s[2 * e]     += __builtin_bit_cast(float, v[j][e] << 16);
      s[2 * e + 1] += __builtin_bit_cast(float, v[j][e] & 0xffff0000u);
    }
  u32x4 o;
#pragma unroll
  for (int e = 0; e < 4; ++e) o[e] = cvtpk(s[2 * e], s[2 * e + 1]);
  const int q = lane & 15, g = lane >> 4;
  *(u32x4*)(out + ((size_t)(b * SQ + 16 * qt + q) * 1024 + br * 512 + 128 * w + 32 * g + 8 * r)) = o;
}

extern "C" void kernel_launch(void* const* d_in, const int* in_sizes, int n_in,
                              void* d_out, int out_size, void* d_ws, size_t ws_size,
                              hipStream_t stream) {
  (void)in_sizes; (void)n_in; (void)out_size; (void)ws_size;
  hipFuncSetAttribute((const void*)gqa_fused_kernel, hipFuncAttributeMaxDynamicSharedMemorySize, LDS_BYTES);
  unsigned short* slab = (unsigned short*)d_ws;
  hipLaunchKernelGGL(gqa_fused_kernel, dim3(256), dim3(512), LDS_BYTES, stream,
                     (const unsigned short*)d_in[0], (const unsigned short*)d_in[1], (const unsigned short*)d_in[2],
                     (const unsigned short*)d_in[3], (const unsigned short*)d_in[4],
                     (const unsigned short*)d_in[5], (const unsigned short*)d_in[6], (const unsigned short*)d_in[7],
                     (const unsigned short*)d_in[8], (const unsigned short*)d_in[9], slab);
  hipLaunchKernelGGL(gqa_reduce_kernel, dim3(384), dim3(256), 0, stream, (const unsigned short*)slab, (unsigned short*)d_out);
}
```

```cpp
#include <hip/hip_runtime.h>
#include <hip/hip_bf16.h>

typedef __bf16   bf16x8 __attribute__((ext_vector_type(8)));
typedef __bf16   bf16x2 __attribute__((ext_vector_type(2)));
typedef short    s16x4  __attribute__((ext_vector_type(4)));
typedef float    f32x4  __attribute__((ext_vector_type(4)));
typedef float    f32x2  __attribute__((ext_vector_type(2)));
typedef unsigned u32x4  __attribute__((ext_vector_type(4)));
typedef unsigned u32x2  __attribute__((ext_vector_type(2)));
#define LAS __attribute__((address_space(3)))
#define DI __device__ __forceinline__

#define STAMP(i) do {} while (0)
#define STAMP_DECL do {} while (0)
#define STAMP_FLUSH do {} while (0)

namespace {
constexpr int NB = 16, SQ = 48, DM = 512, HQ = 32, HKV = 8, SK = 512, DH = 128;
constexpr int XO_BYTES = SQ * DM * 2;
constexpr int SLOT = 16384, NSLOT = 7;
constexpr int RING = XO_BYTES;
constexpr int LDS_BYTES = XO_BYTES + NSLOT * SLOT;
constexpr float CSC = 0.08838834764831845f * 1.4426950408889634f;
constexpr float THR = 8.0f;
constexpr size_t SLAB_F = (size_t)SQ * DM;

DI unsigned cvtpk(float lo, float hi) {
  f32x2 v = {lo, hi}; bf16x2 b = __builtin_convertvector(v, bf16x2); return __builtin_bit_cast(unsigned, b);
}
DI f32x4 mfma(u32x4 a, u32x4 b, f32x4 c) {
  return __builtin_amdgcn_mfma_f32_16x16x32_bf16(__builtin_bit_cast(bf16x8, a), __builtin_bit_cast(bf16x8, b), c, 0, 0, 0);
}
DI void bar() { __builtin_amdgcn_s_barrier(); }
#define WAIT_L0() asm volatile("s_waitcnt lgkmcnt(0)" ::: "memory")
#define WAIT_V(n) asm volatile("s_waitcnt vmcnt(" #n ")" ::: "memory")
#define SB0() __builtin_amdgcn_sched_barrier(0)

DI __amdgpu_buffer_rsrc_t mk_rsrc(const void* p, unsigned bytes) {
  return __builtin_amdgcn_make_buffer_rsrc((void*)p, (short)0, (int)bytes, 0x00020000);
}
#define KV_AUX 0
template <int AUX = 0>
DI void dma16(__amdgpu_buffer_rsrc_t rs, char* lds_dst, unsigned voff, unsigned soff) {
  __builtin_amdgcn_raw_ptr_buffer_load_lds(rs, (LAS void*)lds_dst, 16, (int)voff, (int)soff, 0, AUX);
}

template <unsigned WSTRIDE>
DI void load_w(u32x4 (&w)[8], __amdgpu_buffer_rsrc_t rs, unsigned voff, int ks) {
#pragma unroll
  for (int j = 0; j < 8; ++j)
    w[j] = __builtin_amdgcn_raw_buffer_load_b128(rs, (int)voff, (int)((unsigned)(32 * ks + j) * WSTRIDE), 0);
}
DI void perm_d(u32x4& f0, u32x4& f1, const u32x4 (&w)[8], int d) {
#pragma unroll
  for (int p = 0; p < 4; ++p) {
    f0[p] = __builtin_amdgcn_perm(w[2 * p + 1][d], w[2 * p][d], 0x05040100u);
    f1[p] = __builtin_amdgcn_perm(w[2 * p + 1][d], w[2 * p][d], 0x07060302u);
  }
}
DI void mma_d(f32x4 (&acc)[3][8], const u32x4& f0, const u32x4& f1, const u32x4 (&xb)[3], int d) {
#pragma unroll
  for (int qt = 0; qt < 3; ++qt) {
    acc[qt][2 * d]     = mfma(f0, xb[qt], acc[qt][2 * d]);
    acc[qt][2 * d + 1] = mfma(f1, xb[qt], acc[qt][2 * d + 1]);
  }
}
DI void read_x(u32x4 (&xb)[3], const char* lds, unsigned addr) {
#pragma unroll
  for (int qt = 0; qt < 3; ++qt) xb[qt] = *(const u32x4*)(lds + addr + qt * 16384);
}
template <unsigned WSTRIDE, int I, int NS>
DI void gemm_step(f32x4 (&acc)[3][8], u32x4 (&w)[8], u32x4 (&fa)[2], u32x4 (&fb)[2], u32x4 (&xc)[3], u32x4 (&xn)[3],
                  __amdgpu_buffer_rsrc_t rs, unsigned voff, const char* lds, unsigned xbase, int rot) {
  perm_d(fa[0], fa[1], w, 0);
  if (I > 0) mma_d(acc, fb[0], fb[1], xn, 3);
  SB0();
  if (I + 1 < NS) read_x(xn, lds, xbase ^ (unsigned)(((I + 1 + rot) & 15) << 6));
  perm_d(fb[0], fb[1], w, 1);
  mma_d(acc, fa[0], fa[1], xc, 0);
  SB0();
  perm_d(fa[0], fa[1], w, 2);
  mma_d(acc, fb[0], fb[1], xc, 1);
  SB0();
  perm_d(fb[0], fb[1], w, 3);
  mma_d(acc, fa[0], fa[1], xc, 2);
  SB0();
  if (I + 3 < NS) load_w<WSTRIDE>(w, rs, voff, (I + 3 + rot) & 15);
  SB0();
  if (I == NS - 1) mma_d(acc, fb[0], fb[1], xc, 3);
}
template <unsigned WSTRIDE, int NS>
DI void gemm_main(f32x4 (&acc)[3][8], u32x4 (&w0)[8], u32x4 (&w1)[8], u32x4 (&w2)[8], __amdgpu_buffer_rsrc_t rs, unsigned voff, const char* lds, unsigned xbase, int rot) {
  u32x4 fa[2], fb[2], x0[3], x1[3];
  read_x(x0, lds, xbase ^ (unsigned)(rot << 6));
#define GSA(i, W) gemm_step<WSTRIDE, i, NS>(acc, W, fa, fb, x0, x1, rs, voff, lds, xbase, rot);
#define GSB(i, W) gemm_step<WSTRIDE, i, NS>(acc, W, fa, fb, x1, x0, rs, voff, lds, xbase, rot);
  GSA(0, w0) GSB(1, w1) GSA(2, w2) GSB(3, w0) GSA(4, w1) GSB(5, w2) GSA(6, w0) GSB(7, w1)
  if constexpr (NS == 16) {
    GSA(8, w2) GSB(9, w0) GSA(10, w1) GSB(11, w2) GSA(12, w0) GSB(13, w1) GSA(14, w2) GSB(15, w0)
  }
#undef GSA
#undef GSB
}
}

__global__ __launch_bounds__(512, 2) void gqa_fused_kernel(
    const unsigned short* __restrict__ xA, const unsigned short* __restrict__ kA, const unsigned short* __restrict__ vA,
    const unsigned short* __restrict__ wqA, const unsigned short* __restrict__ woA,
    const unsigned short* __restrict__ xB, const unsigned short* __restrict__ kB, const unsigned short* __restrict__ vB,
    const unsigned short* __restrict__ wqB, const unsigned short* __restrict__ woB,
    unsigned short* __restrict__ slab) {
  extern __shared__ __attribute__((aligned(16))) char lds[];
  STAMP_DECL;
  STAMP(0);
  const int tid = threadIdx.x, lane = tid & 63;
  const int wid = __builtin_amdgcn_readfirstlane(tid >> 6);
  const int li = lane & 15, g = lane >> 4;
  const int blk = blockIdx.x, xc = blk & 7, idx = blk >> 3;
  const int br = xc >> 2, kvh = 2 * (xc & 3) + (idx & 1), b = idx >> 1;
  const unsigned short* x  = (br ? xB : xA) + (size_t)b * SQ * DM;
  const unsigned short* kp = (br ? kB : kA) + ((size_t)b * HKV + kvh) * SK * DH;
  const unsigned short* vp = (br ? vB : vA) + ((size_t)b * HKV + kvh) * SK * DH;
  const unsigned short* wq = (br ? wqB : wqA) + (size_t)kvh * 512;
  const unsigned short* wo = (br ? woB : woA) + (size_t)kvh * 512 * DM;
  const __amdgpu_buffer_rsrc_t rsK = mk_rsrc(kp, SK * DH * 2), rsV = mk_rsrc(vp, SK * DH * 2);
  const __amdgpu_buffer_rsrc_t rsWo = mk_rsrc(wo, 512 * DM * 2);

  const unsigned xbase = (unsigned)(li * 1024 + 16 * (g ^ li));
  const int rot = b;

  const int h = wid & 3, kh = wid >> 2;
  u32x4 qf[3][4];

  auto dma_tile = [&](int u, int p0, int np) {
    char* dst = lds + RING + (u % NSLOT) * SLOT;
    const int t = u >> 1;
#pragma unroll
    for (int i = 0; i < np; ++i) {
      const int p = p0 + i;
      const int row = 4 * p + g;
      if (u & 1) dma16<KV_AUX>(rsV, dst + p * 1024, (unsigned)(g * 256 + 16 * (li ^ (2 * (row & 7)))), (unsigned)((t * 64 + 4 * p) * 256));
      else       dma16<KV_AUX>(rsK, dst + p * 1024, (unsigned)(g * 256 + 16 * (li ^ (row & 15))), (unsigned)((t * 64 + 4 * p) * 256));
    }
  };

  if (wid >= 4) {
    const __amdgpu_buffer_rsrc_t rsX = mk_rsrc(x, SQ * DM * 2);
    const int w4 = wid - 4;
#pragma unroll
    for (int i = 0; i < 12; ++i) {
      const int row = w4 * 12 + i;
      dma16(rsX, lds + row * 1024, (unsigned)(16 * (lane ^ (row & 15))), (unsigned)(row * 1024));
    }
    STAMP(1);
    WAIT_V(0);
    STAMP(2);
    bar();
#pragma unroll
    for (int u = 0; u < NSLOT; ++u) dma_tile(u, w4 * 4, 4);
    bar();
    STAMP(3);
  } else {
    const __amdgpu_buffer_rsrc_t rsW = mk_rsrc(wq, (unsigned)(DM * HQ * DH * 2 - kvh * 1024));
    const int cg = 4 * (li & 3) + (li >> 2);
    const unsigned voff = (unsigned)(8 * g * 8192 + wid * 256 + 16 * cg);
    u32x4 w0[8], w1[8], w2[8];
    load_w<8192>(w0, rsW, voff, rot);
    load_w<8192>(w1, rsW, voff, (rot + 1) & 15);
    load_w<8192>(w2, rsW, voff, (rot + 2) & 15);
    f32x4 acc[3][8];
#pragma unroll
    for (int qt = 0; qt < 3; ++qt)
#pragma unroll
      for (int c = 0; c < 8; ++c) acc[qt][c] = f32x4{0.f, 0.f, 0.f, 0.f};
    STAMP(1);
    bar();
    STAMP(2);
    gemm_main<8192, 16>(acc, w0, w1, w2, rsW, voff, lds, xbase, rot);
    STAMP(3);
#pragma unroll
    for (int qt = 0; qt < 3; ++qt)
#pragma unroll
      for (int s = 0; s < 4; ++s) {
        u32x4 f;
#pragma unroll
        for (int p = 0; p < 4; ++p) f[p] = cvtpk(acc[qt][2 * p][s] * CSC, acc[qt][2 * p + 1][s] * CSC);
        qf[qt][s] = f;
      }
    WAIT_L0();
    bar();
#pragma unroll
    for (int qt = 0; qt < 3; ++qt)
#pragma unroll
      for (int s = 0; s < 4; ++s) *(u32x4*)(lds + wid * 12288 + (qt * 4 + s) * 1024 + lane * 16) = qf[qt][s];
    WAIT_L0();
  }

  unsigned ka[4];
#pragma unroll
  for (int s = 0; s < 4; ++s) ka[s] = (unsigned)(RING + (kh * 32 + li) * 256 + 16 * ((4 * s + g) ^ li));
  const int vr = 4 * g + (li >> 2);
  const unsigned va = (unsigned)(RING + (kh * 32 + vr) * 256 + 32 * (vr & 7) + 8 * (li & 3)) ^ (unsigned)(128 * kh);

  unsigned dko[2], dvo[2];
#pragma unroll
  for (int i = 0; i < 2; ++i) {
    const int row = 8 * wid + 4 * i + g;
    dko[i] = (unsigned)(g * 256 + 16 * (li ^ (row & 15)));
    dvo[i] = (unsigned)(g * 256 + 16 * (li ^ (2 * (row & 7))));
  }
  f32x4 oacc[8][3];
#pragma unroll
  for (int dt = 0; dt < 8; ++dt)
#pragma unroll
    for (int qt = 0; qt < 3; ++qt) oacc[dt][qt] = f32x4{0.f, 0.f, 0.f, 0.f};
  float mS[3], lsum[3];
  f32x4 negm[3];
#pragma unroll
  for (int qt = 0; qt < 3; ++qt) { mS[qt] = 0.f; lsum[qt] = 0.f; negm[qt] = f32x4{0.f, 0.f, 0.f, 0.f}; }

  auto qk = [&](f32x4 (&S)[2][3], unsigned ks_off) {
#pragma unroll
    for (int kt = 0; kt < 2; ++kt) {
#pragma unroll
      for (int s = 0; s < 4; ++s) {
        const u32x4 kf = *(const u32x4*)(lds + ka[s] + ks_off + kt * 4096);
#pragma unroll
        for (int qt = 0; qt < 3; ++qt) S[kt][qt] = mfma(kf, qf[qt][s], s == 0 ? negm[qt] : S[kt][qt]);
      }
    }
  };
  auto remax = [&](f32x4 (&S)[2][3], bool first) {
#pragma unroll
    for (int qt = 0; qt < 3; ++qt) {
      float m = fmaxf(fmaxf(fmaxf(S[0][qt][0] + 0.f, S[0][qt][1] + 0.f), fmaxf(S[0][qt][2] + 0.f, S[0][qt][3] + 0.f)),
                      fmaxf(fmaxf(S[1][qt][0] + 0.f, S[1][qt][1] + 0.f), fmaxf(S[1][qt][2] + 0.f, S[1][qt][3] + 0.f)));
      m = fmaxf(m, __shfl_xor(m, 16));
      m = fmaxf(m, __shfl_xor(m, 32));
      const float dl = first ? m : fmaxf(m, 0.f);
      mS[qt] += dl;
      negm[qt] = f32x4{-mS[qt], -mS[qt], -mS[qt], -mS[qt]};
      S[0][qt] -= dl; S[1][qt] -= dl;
      if (!first) {
        const float al = __builtin_amdgcn_exp2f(-dl);
        lsum[qt] *= al;
#pragma unroll
        for (int dt = 0; dt < 8; ++dt) oacc[dt][qt] *= al;
      }
    }
  };
  auto exp_sum = [&](f32x4 (&S)[2][3], float (&ps)[3]) {
#pragma unroll
    for (int qt = 0; qt < 3; ++qt) {
#pragma unroll
      for (int j = 0; j < 8; ++j) S[j >> 2][qt][j & 3] = __builtin_amdgcn_exp2f(S[j >> 2][qt][j & 3]);
      float a = S[0][qt][0];
#pragma unroll
      for (int j = 1; j < 8; ++j) a += S[j >> 2][qt][j & 3];
      ps[qt] = a;
    }
  };
  auto pack = [&](u32x4 (&pf)[3], const f32x4 (&P)[2][3]) {
#pragma unroll
    for (int qt = 0; qt < 3; ++qt)
#pragma unroll
      for (int e = 0; e < 4; ++e) pf[qt][e] = cvtpk(P[e >> 1][qt][2 * (e & 1)], P[e >> 1][qt][2 * (e & 1) + 1]);
  };
  auto v_read = [&](s16x4 (&lo)[4], s16x4 (&hi)[4], unsigned vs_off, int dh) {
#pragma unroll
    for (int i = 0; i < 4; ++i) {
      const unsigned a = (va + vs_off) ^ (unsigned)(32 * (dh * 4 + i));
      asm volatile("ds_read_b64_tr_b16 %0, %1" : "=&v"(lo[i]) : "v"(a) : "memory");
      asm volatile("ds_read_b64_tr_b16 %0, %1 offset:4096" : "=&v"(hi[i]) : "v"(a) : "memory");
    }
  };
  auto v_wait = [&](u32x4 (&vf)[4], s16x4 (&lo)[4], s16x4 (&hi)[4]) {
    asm volatile("s_waitcnt lgkmcnt(0)" : "+v"(lo[0]), "+v"(lo[1]), "+v"(lo[2]), "+v"(lo[3]), "+v"(hi[0]), "+v"(hi[1]), "+v"(hi[2]), "+v"(hi[3]) :: "memory");
#pragma unroll
    for (int i = 0; i < 4; ++i) {
      const u32x2 l2 = __builtin_bit_cast(u32x2, lo[i]), h2 = __builtin_bit_cast(u32x2, hi[i]);
      vf[i] = u32x4{l2[0], l2[1], h2[0], h2[1]};
    }
  };
  auto pv_mma = [&](const u32x4 (&vf)[4], const u32x4 (&pf)[3], int dh) {
#pragma unroll
    for (int i = 0; i < 4; ++i)
#pragma unroll
      for (int qt = 0; qt < 3; ++qt) oacc[dh * 4 + i][qt] = mfma(vf[i], pf[qt], oacc[dh * 4 + i][qt]);
  };
  auto pv = [&](const u32x4 (&pf)[3], unsigned vs_off) {
    u32x4 vf[4]; s16x4 lo[4], hi[4];
    v_read(lo, hi, vs_off, 0); v_wait(vf, lo, hi); pv_mma(vf, pf, 0);
    v_read(lo, hi, vs_off, 1); v_wait(vf, lo, hi); pv_mma(vf, pf, 1);
  };
  auto k_read = [&](u32x4 (&kf)[4], unsigned ks_off, int kt) {
#pragma unroll
    for (int s = 0; s < 4; ++s) kf[s] = *(const u32x4*)(lds + ka[s] + ks_off + kt * 4096);
  };
  auto qk_mma = [&](f32x4 (&S)[3], const u32x4 (&kf)[4]) {
#pragma unroll
    for (int s = 0; s < 4; ++s)
#pragma unroll
      for (int qt = 0; qt < 3; ++qt) S[qt] = mfma(kf[s], qf[qt][s], s == 0 ? negm[qt] : S[qt]);
  };
  auto exp12 = [&](f32x4 (&S)[3]) {
#pragma unroll
    for (int qt = 0; qt < 3; ++qt)
#pragma unroll
      for (int j = 0; j < 4; ++j) S[qt][j] = __builtin_amdgcn_exp2f(S[qt][j]);
  };
#define SGB(mask, n) __builtin_amdgcn_sched_group_barrier(mask, n, 0)
#define TICK(i) do {} while (0)
#define TICK0() do {} while (0)

  auto qk1 = [&](f32x4& S, const u32x4 (&kf)[4], int qt) {
#pragma unroll
    for (int s = 0; s < 4; ++s) S = mfma(kf[s], qf[qt][s], s == 0 ? negm[qt] : S);
  };
  auto exp4 = [&](f32x4& S) {
#pragma unroll
    for (int j = 0; j < 4; ++j) S[j] = __builtin_amdgcn_exp2f(S[j]);
  };
  auto pack1 = [&](u32x4& pf, const f32x4& P0, const f32x4& P1) {
    pf[0] = cvtpk(P0[0], P0[1]); pf[1] = cvtpk(P0[2], P0[3]); pf[2] = cvtpk(P1[0], P1[1]); pf[3] = cvtpk(P1[2], P1[3]);
  };
  auto sum8 = [&](const f32x4& P0, const f32x4& P1) {
    float a = P0[0];
    a += P0[1]; a += P0[2]; a += P0[3]; a += P1[0]; a += P1[1]; a += P1[2]; a += P1[3];
    return a;
  };

  u32x4 pf[3];
  WAIT_V(24);
  WAIT_L0();
  bar();
  if (wid >= 4) {
#pragma unroll
    for (int qt = 0; qt < 3; ++qt)
#pragma unroll
      for (int s = 0; s < 4; ++s) qf[qt][s] = *(const u32x4*)(lds + h * 12288 + (qt * 4 + s) * 1024 + lane * 16);
  }
  {
    float ps[3]; f32x4 P[2][3];
    qk(P, 0u);
    remax(P, true);
    exp_sum(P, ps);
    pack(pf, P);
#pragma unroll
    for (int qt = 0; qt < 3; ++qt) lsum[qt] = ps[qt];
  }
  STAMP(4);
  TICK0();
  int slV = 1;
#pragma nounroll
  for (int t = 0; t < 7; ++t) {
    WAIT_V(6);
    WAIT_L0();
    bar();
    TICK(0);
    const int slK = slV == 6 ? 0 : slV + 1;
    const unsigned ko = (unsigned)(slK * SLOT), vo = (unsigned)(slV * SLOT);
    f32x4 S[2][3];
    u32x4 pn[3], kf0[4], kf1[4], vf0[4], vf1[4];
    s16x4 l0[4], h0[4], l1[4], h1[4];
    float ps[3];
    k_read(kf0, ko, 0);
    k_read(kf1, ko, 1);
    SB0();
    {
      const int fK = slV >= 2 ? slV - 2 : slV + 5, fV = slV >= 1 ? slV - 1 : 6;
      char* d1 = lds + RING + fK * SLOT + wid * 2048;
      char* d0 = lds + RING + fV * SLOT + wid * 2048;
      const unsigned so = (unsigned)(((t + 3) * 64 + 8 * wid) * 256);
      if (t != 5) {
        dma16<KV_AUX>(rsK, d1, dko[0], so);
        dma16<KV_AUX>(rsK, d1 + 1024, dko[1], so + 1024);
        dma16<KV_AUX>(rsV, d0, dvo[0], so);
        dma16<KV_AUX>(rsV, d0 + 1024, dvo[1], so + 1024);
      } else {
        const unsigned sw = (unsigned)(b * 32768 + wid * 4096);
        dma16(rsWo, d1, dko[0], sw);
        dma16(rsWo, d1 + 1024, dko[0], sw + 1024);
        dma16(rsWo, d0, dko[0], sw + 2048);
        dma16(rsWo, d0 + 1024, dko[0], sw + 3072);
      }
    }
    SB0();
    TICK(2);
    qk1(S[0][0], kf0, 0); qk1(S[0][1], kf0, 1); qk1(S[0][2], kf0, 2);
    exp4(S[0][0]); exp4(S[0][1]);
    SGB(0x008, 4);
#pragma unroll
    for (int i = 0; i < 8; ++i) { SGB(0x008, 1); SGB(0x400, 1); }
    SB0();
    TICK(3);
    v_read(l0, h0, vo, 0);
    qk1(S[1][0], kf1, 0); qk1(S[1][1], kf1, 1); qk1(S[1][2], kf1, 2);
    exp4(S[0][2]); exp4(S[1][0]); exp4(S[1][1]);
#pragma unroll
    for (int i = 0; i < 12; ++i) { SGB(0x008, 1); SGB(0x400, 1); }
    SB0();
    TICK(4);
    v_wait(vf0, l0, h0);
    v_read(l1, h1, vo, 1);
    pv_mma(vf0, pf, 0);
    exp4(S[1][2]);
    pack1(pn[0], S[0][0], S[1][0]); pack1(pn[1], S[0][1], S[1][1]);
    ps[0] = sum8(S[0][0], S[1][0]);
#pragma unroll
    for (int i = 0; i < 4; ++i) { SGB(0x008, 1); SGB(0x400, 1); }
#pragma unroll
    for (int i = 0; i < 8; ++i) { SGB(0x008, 1); SGB(0x002, 2); }
    SB0();
    TICK(5);
    v_wait(vf1, l1, h1);
    pv_mma(vf1, pf, 1);
    pack1(pn[2], S[0][2], S[1][2]);
    ps[1] = sum8(S[0][1], S[1][1]); ps[2] = sum8(S[0][2], S[1][2]);
#pragma unroll
    for (int i = 0; i < 10; ++i) { SGB(0x008, 1); SGB(0x002, 2); }
    SB0();
    TICK(6);
    if (__any((ps[0] > 65536.f) | (ps[1] > 65536.f) | (ps[2] > 65536.f))) {
      qk(S, ko);
      remax(S, false);
      exp_sum(S, ps);
      pack(pn, S);
    }
#pragma unroll
    for (int qt = 0; qt < 3; ++qt) { lsum[qt] += ps[qt]; pf[qt] = pn[qt]; }
    slV = slV >= 5 ? slV - 5 : slV + 2;
    TICK(7);
  }
  {
    WAIT_V(0);
    WAIT_L0();
    bar();
    STAMP(11);
    pv(pf, (unsigned)(slV * SLOT));
  }

  STAMP(12);
  int tz = threadIdx.x; asm volatile("" : "+v"(tz));
  const int lane2 = tz & 63, li2 = lane2 & 15, g2 = lane2 >> 4;
#define lane lane2
#define li li2
#define g g2
  const unsigned xbase2 = (unsigned)(li * 1024 + 16 * (g ^ li));
  u32x4 w0[8], w1[8], w2[8];
  const unsigned voffo = (unsigned)(8 * g * 1024 + h * 256 + 16 * (li ^ (2 * kh)));
  const int rot3 = (rot + 8 * kh) & 15;
  load_w<1024>(w0, rsWo, voffo, rot3);
  load_w<1024>(w1, rsWo, voffo, (rot3 + 1) & 15);
  STAMP(5);
#pragma unroll
  for (int qt = 0; qt < 3; ++qt) {
    float l = lsum[qt];
    l += __shfl_xor(l, 16);
    l += __shfl_xor(l, 32);
    lsum[qt] = l;
  }
  STAMP(6);
  WAIT_L0();
  bar();
  STAMP(7);
  {
    char* my = lds + RING + wid * 12288;
#pragma unroll
    for (int i = 0; i < 4; ++i)
#pragma unroll
      for (int qt = 0; qt < 3; ++qt) *(f32x4*)(my + (i * 3 + qt) * 1024 + lane * 16) = oacc[4 + i][qt];
    float* ml = (float*)(lds + RING + 8 * 12288) + wid * 384;
#pragma unroll
    for (int qt = 0; qt < 3; ++qt) { ml[(2 * qt) * 64 + lane] = mS[qt]; ml[(2 * qt + 1) * 64 + lane] = lsum[qt]; }
  }
  WAIT_L0();
  STAMP(8);
  bar();
  STAMP(9);
  {
    const int pw = wid ^ 4;
    const char* ot = lds + RING + pw * 12288;
    const float* ml = (const float*)(lds + RING + 8 * 12288) + pw * 384;
    float mo[3], lo_[3];
    f32x4 o[4][3];
#pragma unroll
    for (int qt = 0; qt < 3; ++qt) { mo[qt] = ml[(2 * qt) * 64 + lane]; lo_[qt] = ml[(2 * qt + 1) * 64 + lane]; }
#pragma unroll
    for (int i = 0; i < 4; ++i)
#pragma unroll
      for (int qt = 0; qt < 3; ++qt) o[i][qt] = *(const f32x4*)(ot + (i * 3 + qt) * 1024 + lane * 16);
    SB0();
#pragma unroll
    for (int qt = 0; qt < 3; ++qt) {
      const float M = fmaxf(mS[qt], mo[qt]);
      const float wa = __builtin_amdgcn_exp2f(mS[qt] - M), wb = __builtin_amdgcn_exp2f(mo[qt] - M);
      const float inv = __builtin_amdgcn_rcpf(wa * lsum[qt] + wb * lo_[qt]);
      const float fa = wa * inv, fb = wb * inv;
      const int row = 16 * qt + li;
#pragma unroll
      for (int i = 0; i < 4; ++i) {
        const f32x4 mine = oacc[i][qt];
        const int dt = kh * 4 + i;
        u32x2 r;
        r[0] = cvtpk(mine[0] * fa + o[i][qt][0] * fb, mine[1] * fa + o[i][qt][1] * fb);
        r[1] = cvtpk(mine[2] * fa + o[i][qt][2] * fb, mine[3] * fa + o[i][qt][3] * fb);
        const int chunk = 16 * h + 2 * dt + (g >> 1);
        *(u32x2*)(lds + row * 1024 + 16 * (chunk ^ li) + 8 * (g & 1)) = r;
      }
    }
  }
  STAMP(10);
  load_w<1024>(w2, rsWo, voffo, (rot3 + 2) & 15);
  WAIT_L0();
  bar();
  STAMP(13);
  {
    f32x4 acc[3][8];
#pragma unroll
    for (int qt = 0; qt < 3; ++qt)
#pragma unroll
      for (int c = 0; c < 8; ++c) acc[qt][c] = f32x4{0.f, 0.f, 0.f, 0.f};
    gemm_main<1024, 8>(acc, w0, w1, w2, rsWo, voffo, lds, xbase2, rot3);
    STAMP(14);
    int tz3 = threadIdx.x; asm volatile("" : "+v"(tz3));
    const int lane3 = tz3 & 63;
    char* my = lds + RING + wid * 12288 + lane3 * 8;
#pragma unroll
    for (int qt = 0; qt < 3; ++qt)
#pragma unroll
      for (int c = 0; c < 8; ++c) *(f32x2*)(my + (qt * 8 + c) * 512) = f32x2{acc[qt][c][2], acc[qt][c][3]};
    WAIT_L0();
    bar();
    const char* ot = lds + RING + (wid ^ 4) * 12288 + lane3 * 8;
    unsigned short* dst = slab + (size_t)(br * 16 + b) * (8 * SLAB_F) + (size_t)kvh * SLAB_F + (size_t)h * 6144 + lane3 * 8;
#pragma unroll
    for (int qt = 0; qt < 3; ++qt) {
      f32x2 o[8];
#pragma unroll
      for (int c = 0; c < 8; ++c) o[c] = *(const f32x2*)(ot + (qt * 8 + c) * 512);
#pragma unroll
      for (int rr = 0; rr < 2; ++rr) {
        u32x4 v;
#pragma unroll
        for (int p2 = 0; p2 < 4; ++p2) v[p2] = cvtpk(acc[qt][2 * p2][rr] + o[2 * p2][rr], acc[qt][2 * p2 + 1][rr] + o[2 * p2 + 1][rr]);
        *(u32x4*)(dst + (qt * 4 + 2 * kh + rr) * 512) = v;
      }
    }
  }
  STAMP(15);
  STAMP_FLUSH;
#undef lane
#undef li
#undef g
}

__global__ __launch_bounds__(256) void gqa_reduce_kernel(const unsigned short* __restrict__ slab, unsigned short* __restrict__ out) {
  const int idx = blockIdx.x * 256 + threadIdx.x;
  const int lane = idx & 63, r = (idx >> 6) & 3;
  const int t2 = idx >> 8;
  const int rest = t2 / 12, wq3 = t2 % 12, w = wq3 / 3, qt = wq3 % 3;
  const int br = rest >> 4, b = rest & 15;
  const unsigned short* src = slab + (size_t)rest * 8 * SLAB_F + (size_t)w * 6144 + (size_t)(qt * 4 + r) * 512 + lane * 8;
  u32x4 v[8];
#pragma unroll
  for (int j = 0; j < 8; ++j) v[j] = *(const u32x4*)(src + (size_t)j * SLAB_F);
  float s[8] = {0.f, 0.f, 0.f, 0.f, 0.f, 0.f, 0.f, 0.f};
#pragma unroll
  for (int j = 0; j < 8; ++j)
#pragma unroll
    for (int e = 0; e < 4; ++e) {
      s[2 * e]     += __builtin_bit_cast(float, v[j][e] << 16);
      s[2 * e + 1] += __builtin_bit_cast(float, v[j][e] & 0xffff0000u);
    }
  u32x4 o;
#pragma unroll
  for (int e = 0; e < 4; ++e) o[e] = cvtpk(s[2 * e], s[2 * e + 1]);
  const int q = lane & 15, g = lane >> 4;
  *(u32x4*)(out + ((size_t)(b * SQ + 16 * qt + q) * 1024 + br * 512 + 128 * w + 32 * g + 8 * r)) = o;
}

extern "C" void kernel_launch(void* const* d_in, const int* in_sizes, int n_in,
                              void* d_out, int out_size, void* d_ws, size_t ws_size,
                              hipStream_t stream) {
  (void)in_sizes; (void)n_in; (void)out_size; (void)ws_size;
  hipFuncSetAttribute((const void*)gqa_fused_kernel, hipFuncAttributeMaxDynamicSharedMemorySize, LDS_BYTES);
  unsigned short* slab = (unsigned short*)d_ws;
  hipLaunchKernelGGL(gqa_fused_kernel, dim3(256), dim3(512), LDS_BYTES, stream,
                     (const unsigned short*)d_in[0], (const unsigned short*)d_in[1], (const unsigned short*)d_in[2],
                     (const unsigned short*)d_in[3], (const unsigned short*)d_in[4],
                     (const unsigned short*)d_in[5], (const unsigned short*)d_in[6], (const unsigned short*)d_in[7],
                     (const unsigned short*)d_in[8], (const unsigned short*)d_in[9], slab);
  hipLaunchKernelGGL(gqa_reduce_kernel, dim3(384), dim3(256), 0, stream, (const unsigned short*)slab, (unsigned short*)d_out);
}
```

```cpp
#include <hip/hip_runtime.h>
#include <hip/hip_bf16.h>

typedef __bf16   bf16x8 __attribute__((ext_vector_type(8)));
typedef __bf16   bf16x2 __attribute__((ext_vector_type(2)));
typedef short    s16x4  __attribute__((ext_vector_type(4)));
typedef float    f32x4  __attribute__((ext_vector_type(4)));
typedef float    f32x2  __attribute__((ext_vector_type(2)));
typedef unsigned u32x4  __attribute__((ext_vector_type(4)));
typedef unsigned u32x2  __attribute__((ext_vector_type(2)));
#define LAS __attribute__((address_space(3)))
#define GAS __attribute__((address_space(1)))
typedef GAS unsigned long long gu64;
#define RLX_AGENT __ATOMIC_RELAXED, __HIP_MEMORY_SCOPE_AGENT
#define DI __device__ __forceinline__

namespace {
constexpr int NB = 16, SQ = 48, DM = 512, HQ = 32, HKV = 8, SK = 512, DH = 128;
constexpr int XO_BYTES = SQ * DM * 2;
constexpr int SLOT = 16384, NSLOT = 7;
constexpr int RING = XO_BYTES;
constexpr int LDS_BYTES = XO_BYTES + NSLOT * SLOT;
constexpr float CSC = 0.08838834764831845f * 1.4426950408889634f;
constexpr size_t SLAB_F = (size_t)SQ * DM;
constexpr size_t CTRL_OFF = (size_t)16 << 20;

DI unsigned cvtpk(float lo, float hi) {
  f32x2 v = {lo, hi}; bf16x2 b = __builtin_convertvector(v, bf16x2); return __builtin_bit_cast(unsigned, b);
}
DI f32x4 mfma(u32x4 a, u32x4 b, f32x4 c) {
  return __builtin_amdgcn_mfma_f32_16x16x32_bf16(__builtin_bit_cast(bf16x8, a), __builtin_bit_cast(bf16x8, b), c, 0, 0, 0);
}
DI void bar() { __builtin_amdgcn_s_barrier(); }
#define WAIT_L0() asm volatile("s_waitcnt lgkmcnt(0)" ::: "memory")
#define WAIT_V(n) asm volatile("s_waitcnt vmcnt(" #n ")" ::: "memory")
#define SB0() __builtin_amdgcn_sched_barrier(0)

DI __amdgpu_buffer_rsrc_t mk_rsrc(const void* p, unsigned bytes) {
  return __builtin_amdgcn_make_buffer_rsrc((void*)p, (short)0, (int)bytes, 0x00020000);
}
#define KV_S0 13
#define KV_S1 13
#define KV_S2 13
#define KV_S3 13
#define KV_AUX 2
template <int AUX = 0>
DI void dma16(__amdgpu_buffer_rsrc_t rs, char* lds_dst, unsigned voff, unsigned soff) {
  __builtin_amdgcn_raw_ptr_buffer_load_lds(rs, (LAS void*)lds_dst, 16, (int)voff, (int)soff, 0, AUX);
}

template <unsigned WSTRIDE>
DI void load_w(u32x4 (&w)[8], __amdgpu_buffer_rsrc_t rs, unsigned voff, int ks) {
#pragma unroll
  for (int j = 0; j < 8; ++j)
    w[j] = __builtin_amdgcn_raw_buffer_load_b128(rs, (int)voff, (int)((unsigned)(32 * ks + j) * WSTRIDE), 0);
}
DI void perm_d(u32x4& f0, u32x4& f1, const u32x4 (&w)[8], int d) {
#pragma unroll
  for (int p = 0; p < 4; ++p) {
    f0[p] = __builtin_amdgcn_perm(w[2 * p + 1][d], w[2 * p][d], 0x05040100u);
    f1[p] = __builtin_amdgcn_perm(w[2 * p + 1][d], w[2 * p][d], 0x07060302u);
  }
}
DI void mma_d(f32x4 (&acc)[3][8], const u32x4& f0, const u32x4& f1, const u32x4 (&xb)[3], int d) {
#pragma unroll
  for (int qt = 0; qt < 3; ++qt) {
    acc[qt][2 * d]     = mfma(f0, xb[qt], acc[qt][2 * d]);
    acc[qt][2 * d + 1] = mfma(f1, xb[qt], acc[qt][2 * d + 1]);
  }
}
DI void read_x(u32x4 (&xb)[3], const char* lds, unsigned addr) {
#pragma unroll
  for (int qt = 0; qt < 3; ++qt) xb[qt] = *(const u32x4*)(lds + addr + qt * 16384);
}
template <unsigned WSTRIDE>
DI void load_w2(u32x4 (&w)[8], __amdgpu_buffer_rsrc_t rs, unsigned voff, int ks, int j0) {
#pragma unroll
  for (int j = j0; j < j0 + 2; ++j)
    w[j] = __builtin_amdgcn_raw_buffer_load_b128(rs, (int)voff, (int)((unsigned)(32 * ks + j) * WSTRIDE), 0);
}
template <unsigned WSTRIDE>
DI void load_w2(u32x4 (&w)[8], __amdgpu_buffer_rsrc_t rs, unsigned voff, int ks, int, int j) {
  w[j] = __builtin_amdgcn_raw_buffer_load_b128(rs, (int)voff, (int)((unsigned)(32 * ks + j) * WSTRIDE), 0);
}
template <unsigned WSTRIDE, int I, int NS>
DI void gemm_step(f32x4 (&acc)[3][8], u32x4 (&w)[8], u32x4 (&wp)[8], u32x4 (&fa)[2], u32x4 (&fb)[2], u32x4 (&xc)[3], u32x4 (&xn)[3],
                  __amdgpu_buffer_rsrc_t rs, unsigned voff, const char* lds, unsigned xbase, int rot, int bk) {
  constexpr bool LD = I >= 1 && I + 2 < NS;
  const int kn = (I + 2 + rot) & 15;
  perm_d(fa[0], fa[1], w, 0);
  if (I > 0) mma_d(acc, fb[0], fb[1], xn, 3);
  if (LD) load_w2<WSTRIDE>(wp, rs, voff, kn, 0);
  SB0();
  if (I + 1 < NS) read_x(xn, lds, xbase ^ (unsigned)(((I + 1 + rot) & 15) << 6));
  perm_d(fb[0], fb[1], w, 1);
  mma_d(acc, fa[0], fa[1], xc, 0);
  if (LD) load_w2<WSTRIDE>(wp, rs, voff, kn, 2);
  SB0();
  perm_d(fa[0], fa[1], w, 2);
  mma_d(acc, fb[0], fb[1], xc, 1);
  if (LD) load_w2<WSTRIDE>(wp, rs, voff, kn, 4);
  SB0();
  perm_d(fb[0], fb[1], w, 3);
  mma_d(acc, fa[0], fa[1], xc, 2);
  if (LD) load_w2<WSTRIDE>(wp, rs, voff, kn, 6);
  SB0();
  if (I == bk) { bar(); SB0(); }
  if (I == NS - 1) mma_d(acc, fb[0], fb[1], xc, 3);
}
template <unsigned WSTRIDE, int NS>
DI void gemm_main(f32x4 (&acc)[3][8], u32x4 (&w0)[8], u32x4 (&w1)[8], u32x4 (&w2)[8], __amdgpu_buffer_rsrc_t rs, unsigned voff, const char* lds, unsigned xbase, int rot, int bk) {
  u32x4 fa[2], fb[2], x0[3], x1[3];
  read_x(x0, lds, xbase ^ (unsigned)(rot << 6));
#define GSA(i, W, WP) gemm_step<WSTRIDE, i, NS>(acc, W, WP, fa, fb, x0, x1, rs, voff, lds, xbase, rot, bk);
#define GSB(i, W, WP) gemm_step<WSTRIDE, i, NS>(acc, W, WP, fa, fb, x1, x0, rs, voff, lds, xbase, rot, bk);
  GSA(0, w0, w2) GSB(1, w1, w0) GSA(2, w2, w1) GSB(3, w0, w2) GSA(4, w1, w0) GSB(5, w2, w1) GSA(6, w0, w2) GSB(7, w1, w0)
  if constexpr (NS == 16) {
    GSA(8, w2, w1) GSB(9, w0, w2) GSA(10, w1, w0) GSB(11, w2, w1) GSA(12, w0, w2) GSB(13, w1, w0) GSA(14, w2, w1) GSB(15, w0, w2)
  }
#undef GSA
#undef GSB
}
}

__global__ __launch_bounds__(512, 2) void gqa_fused_kernel(
    const unsigned short* __restrict__ xA, const unsigned short* __restrict__ kA, const unsigned short* __restrict__ vA,
    const unsigned short* __restrict__ wqA, const unsigned short* __restrict__ woA,
    const unsigned short* __restrict__ xB, const unsigned short* __restrict__ kB, const unsigned short* __restrict__ vB,
    const unsigned short* __restrict__ wqB, const unsigned short* __restrict__ woB,
    unsigned short* slab, unsigned short* __restrict__ out) {
  extern __shared__ __attribute__((aligned(16))) char lds[];
  const int tid = threadIdx.x, lane = tid & 63;
  const int wid = __builtin_amdgcn_readfirstlane(tid >> 6);
  const int li = lane & 15, g = lane >> 4;
  const int blk = blockIdx.x, xc = blk & 7, idx = blk >> 3;
  const int br = xc >> 2, kvh = 2 * (xc & 3) + (idx & 1), b = idx >> 1;
  const unsigned short* x  = (br ? xB : xA) + (size_t)b * SQ * DM;
  const unsigned short* kp = (br ? kB : kA) + ((size_t)b * HKV + kvh) * SK * DH;
  const unsigned short* vp = (br ? vB : vA) + ((size_t)b * HKV + kvh) * SK * DH;
  const unsigned short* wq = (br ? wqB : wqA) + (size_t)kvh * 512;
  const unsigned short* wo = (br ? woB : woA) + (size_t)kvh * 512 * DM;
  const __amdgpu_buffer_rsrc_t rsK = mk_rsrc(kp, SK * DH * 2), rsV = mk_rsrc(vp, SK * DH * 2);
  const __amdgpu_buffer_rsrc_t rsWo = mk_rsrc(wo, 512 * DM * 2);

  const unsigned xbase = (unsigned)(li * 1024 + 16 * (g ^ li));
  gu64* ctrl = (gu64*)((char*)slab + CTRL_OFF) + (size_t)(br * 16 + b) * 128;
  const unsigned long long ew = __hip_atomic_load(ctrl, RLX_AGENT);
  unsigned e_lo, e_hi;
  const int rot = b;
  const int kvs = (b & 3) == 0 ? KV_S0 : (b & 3) == 1 ? KV_S1 : (b & 3) == 2 ? KV_S2 : KV_S3;

  const int h = wid & 3, kh = wid >> 2;
  u32x4 qf[3][4];

  auto dma_tile = [&](int u, int p0, int np) {
    char* dst = lds + RING + (u % NSLOT) * SLOT;
    const int t = u >> 1;
#pragma unroll
    for (int i = 0; i < np; ++i) {
      const int p = p0 + i;
      const int row = 4 * p + g;
      if (u & 1) dma16<KV_AUX>(rsV, dst + p * 1024, (unsigned)(g * 256 + 16 * (li ^ (2 * (row & 7)))), (unsigned)((t * 64 + 4 * p) * 256));
      else       dma16<KV_AUX>(rsK, dst + p * 1024, (unsigned)(g * 256 + 16 * (li ^ ((row + 4) & 15))), (unsigned)((t * 64 + 4 * p) * 256));
    }
  };

  if (wid >= 4) {
    const __amdgpu_buffer_rsrc_t rsX = mk_rsrc(x, SQ * DM * 2);
    const int w4 = wid - 4;
#pragma unroll
    for (int i = 0; i < 12; ++i) {
      const int row = w4 * 12 + i;
      dma16(rsX, lds + row * 1024, (unsigned)(16 * (lane ^ (row & 15))), (unsigned)(row * 1024));
    }
    dma_tile(0, w4 * 4, 4);
    WAIT_V(4);
    { const unsigned long long e = ew + 1; e_lo = __builtin_amdgcn_readfirstlane((unsigned)e); e_hi = __builtin_amdgcn_readfirstlane((unsigned)(e >> 32)); }
    bar();
    bar();
#pragma unroll
    for (int u = 1; u < 3; ++u) dma_tile(u, w4 * 4, 4);
    bar();
  } else {
    const __amdgpu_buffer_rsrc_t rsW = mk_rsrc(wq, (unsigned)(DM * HQ * DH * 2 - kvh * 1024));
    const unsigned voff = (unsigned)(8 * g * 8192 + wid * 256 + 16 * li);
    u32x4 w0[8], w1[8], w2[8];
    load_w<8192>(w0, rsW, voff, rot);
    load_w<8192>(w1, rsW, voff, (rot + 1) & 15);
    load_w<8192>(w2, rsW, voff, (rot + 2) & 15);
    f32x4 acc[3][8];
#pragma unroll
    for (int qt = 0; qt < 3; ++qt)
#pragma unroll
      for (int c = 0; c < 8; ++c) acc[qt][c] = f32x4{0.f, 0.f, 0.f, 0.f};
    bar();
    { const unsigned long long e = ew + 1; e_lo = __builtin_amdgcn_readfirstlane((unsigned)e); e_hi = __builtin_amdgcn_readfirstlane((unsigned)(e >> 32)); }
    gemm_main<8192, 16>(acc, w0, w1, w2, rsW, voff, lds, xbase, rot, kvs);
#pragma unroll
    for (int qt = 0; qt < 3; ++qt)
#pragma unroll
      for (int s = 0; s < 4; ++s) {
        u32x4 f;
#pragma unroll
        for (int p = 0; p < 4; ++p) f[p] = cvtpk(acc[qt][2 * p][s], acc[qt][2 * p + 1][s]);
        qf[qt][s] = f;
      }
    WAIT_L0();
    bar();
#pragma unroll
    for (int qt = 0; qt < 3; ++qt)
#pragma unroll
      for (int s = 0; s < 4; ++s) *(u32x4*)(lds + wid * 12288 + (qt * 4 + s) * 1024 + lane * 16) = qf[qt][s];
    WAIT_L0();
  }

  const unsigned ka0 = (unsigned)(RING + (kh * 32 + li) * 256 + 16 * ((4 * g) ^ ((li + 4) & 15)));
  const int vr = 4 * g + (li >> 2);
  const unsigned va = (unsigned)(RING + (kh * 32 + vr) * 256 + 32 * (vr & 7) + 8 * (li & 3)) ^ (unsigned)(128 * kh);

  const unsigned dko0 = (unsigned)(g * 256 + 16 * (li ^ ((8 * wid + g + 4) & 15))), dvo0 = (unsigned)(g * 256 + 16 * (li ^ (2 * g)));
  f32x4 oacc[8][3];
#pragma unroll
  for (int dt = 0; dt < 8; ++dt)
#pragma unroll
    for (int qt = 0; qt < 3; ++qt) oacc[dt][qt] = f32x4{0.f, 0.f, 0.f, 0.f};
  float lsum[3];
  float negm[3];
#pragma unroll
  for (int qt = 0; qt < 3; ++qt) { lsum[qt] = 0.f; negm[qt] = 0.f; }
  const f32x4 zero4 = {0.f, 0.f, 0.f, 0.f};

  auto qk = [&](f32x4 (&S)[2][3], unsigned ks_off) {
#pragma unroll
    for (int kt = 0; kt < 2; ++kt) {
#pragma unroll
      for (int s = 0; s < 4; ++s) {
        const u32x4 kf = *(const u32x4*)(lds + ((ka0 + ks_off) ^ (unsigned)(16 * s)) + kt * 4096);
#pragma unroll
        for (int qt = 0; qt < 3; ++qt) S[kt][qt] = mfma(kf, qf[qt][s], s == 0 ? zero4 : S[kt][qt]);
        SB0();
      }
#pragma unroll
      for (int qt = 0; qt < 3; ++qt)
#pragma unroll
        for (int j = 0; j < 4; ++j) S[kt][qt][j] = fmaf(S[kt][qt][j], CSC, negm[qt]);
    }
  };
  auto remax = [&](f32x4 (&S)[2][3], bool first) {
#pragma unroll
    for (int qt = 0; qt < 3; ++qt) {
      float m = fmaxf(fmaxf(fmaxf(S[0][qt][0], S[0][qt][1]), fmaxf(S[0][qt][2], S[0][qt][3])),
                      fmaxf(fmaxf(S[1][qt][0], S[1][qt][1]), fmaxf(S[1][qt][2], S[1][qt][3])));
      m = fmaxf(m, __shfl_xor(m, 16));
      m = fmaxf(m, __shfl_xor(m, 32));
      const float dl = first ? m : fmaxf(m, 0.f);
      negm[qt] -= dl;
      S[0][qt] -= dl; S[1][qt] -= dl;
      if (!first) {
        const float al = __builtin_amdgcn_exp2f(-dl);
        lsum[qt] *= al;
#pragma unroll
        for (int dt = 0; dt < 8; ++dt) oacc[dt][qt] *= al;
      }
    }
  };
  auto exp_sum = [&](f32x4 (&S)[2][3], float (&ps)[3]) {
#pragma unroll
    for (int qt = 0; qt < 3; ++qt) {
#pragma unroll
      for (int j = 0; j < 8; ++j) S[j >> 2][qt][j & 3] = __builtin_amdgcn_exp2f(S[j >> 2][qt][j & 3]);
      float a = S[0][qt][0];
#pragma unroll
      for (int j = 1; j < 8; ++j) a += S[j >> 2][qt][j & 3];
      ps[qt] = a;
    }
  };
  auto pack = [&](u32x4 (&pf)[3], const f32x4 (&P)[2][3]) {
#pragma unroll
    for (int qt = 0; qt < 3; ++qt)
#pragma unroll
      for (int e = 0; e < 4; ++e) pf[qt][e] = cvtpk(P[e >> 1][qt][2 * (e & 1)], P[e >> 1][qt][2 * (e & 1) + 1]);
  };
  auto v_read = [&](s16x4 (&lo)[4], s16x4 (&hi)[4], unsigned vs_off, int dh) {
#pragma unroll
    for (int i = 0; i < 4; ++i) {
      const unsigned a = (va + vs_off) ^ (unsigned)(32 * (dh * 4 + i));
      asm volatile("ds_read_b64_tr_b16 %0, %1" : "=&v"(lo[i]) : "v"(a) : "memory");
      asm volatile("ds_read_b64_tr_b16 %0, %1 offset:4096" : "=&v"(hi[i]) : "v"(a) : "memory");
    }
  };
  auto v_wait = [&](u32x4 (&vf)[4], s16x4 (&lo)[4], s16x4 (&hi)[4]) {
    asm volatile("s_waitcnt lgkmcnt(0)" : "+v"(lo[0]), "+v"(lo[1]), "+v"(lo[2]), "+v"(lo[3]), "+v"(hi[0]), "+v"(hi[1]), "+v"(hi[2]), "+v"(hi[3]) :: "memory");
#pragma unroll
    for (int i = 0; i < 4; ++i) {
      const u32x2 l2 = __builtin_bit_cast(u32x2, lo[i]), h2 = __builtin_bit_cast(u32x2, hi[i]);
      vf[i] = u32x4{l2[0], l2[1], h2[0], h2[1]};
    }
  };
  auto pv_mma = [&](const u32x4 (&vf)[4], const u32x4 (&pf)[3], int dh) {
#pragma unroll
    for (int i = 0; i < 4; ++i)
#pragma unroll
      for (int qt = 0; qt < 3; ++qt) oacc[dh * 4 + i][qt] = mfma(vf[i], pf[qt], oacc[dh * 4 + i][qt]);
  };
  auto pv = [&](const u32x4 (&pf)[3], unsigned vs_off) {
    u32x4 vf[4]; s16x4 lo[4], hi[4];
    v_read(lo, hi, vs_off, 0); v_wait(vf, lo, hi); pv_mma(vf, pf, 0);
    v_read(lo, hi, vs_off, 1); v_wait(vf, lo, hi); pv_mma(vf, pf, 1);
  };
  auto k_read = [&](u32x4 (&kf)[4], unsigned ks_off, int kt) {
#pragma unroll
    for (int s = 0; s < 4; ++s) kf[s] = *(const u32x4*)(lds + ((ka0 + ks_off) ^ (unsigned)(16 * s)) + kt * 4096);
  };
#define SGB(mask, n) __builtin_amdgcn_sched_group_barrier(mask, n, 0)

  auto qk1 = [&](f32x4& S, const u32x4 (&kf)[4], int qt) {
#pragma unroll
    for (int s = 0; s < 4; ++s) S = mfma(kf[s], qf[qt][s], s == 0 ? zero4 : S);
  };
  auto exp4 = [&](f32x4& S, int qt) {
#pragma unroll
    for (int j = 0; j < 4; ++j) S[j] = __builtin_amdgcn_exp2f(fmaf(S[j], CSC, negm[qt]));
  };
  auto pack1 = [&](u32x4& pf, const f32x4& P0, const f32x4& P1) {
    pf[0] = cvtpk(P0[0], P0[1]); pf[1] = cvtpk(P0[2], P0[3]); pf[2] = cvtpk(P1[0], P1[1]); pf[3] = cvtpk(P1[2], P1[3]);
  };
  auto sum8 = [&](const f32x4& P0, const f32x4& P1) {
    float a = P0[0];
    a += P0[1]; a += P0[2]; a += P0[3]; a += P1[0]; a += P1[1]; a += P1[2]; a += P1[3];
    return a;
  };

  u32x4 pf[3];
  if (kh) __builtin_amdgcn_s_setprio(1);
  WAIT_V(8);
  WAIT_L0();
  bar();
  if (wid >= 4) {
#pragma unroll
    for (int qt = 0; qt < 3; ++qt)
#pragma unroll
      for (int s = 0; s < 4; ++s) qf[qt][s] = *(const u32x4*)(lds + h * 12288 + (qt * 4 + s) * 1024 + lane * 16);
  }
  {
    float ps[3]; f32x4 P[2][3];
    qk(P, 0u);
#pragma unroll
    for (int u = 3; u < 6; ++u) dma_tile(u, wid * 2, 2);
    remax(P, true);
    exp_sum(P, ps);
    pack(pf, P);
#pragma unroll
    for (int qt = 0; qt < 3; ++qt) lsum[qt] = ps[qt];
  }
  int slV = 1;
#pragma nounroll
  for (int t = 0; t < 7; ++t) {
    WAIT_V(6);
    WAIT_L0();
    bar();
    const int slK = slV == 6 ? 0 : slV + 1;
    const unsigned ko = (unsigned)(slK * SLOT), vo = (unsigned)(slV * SLOT);
    f32x4 S[2][3];
    u32x4 pn[3], kf0[4], kf1[4], vf0[4], vf1[4];
    s16x4 l0[4], h0[4], l1[4], h1[4];
    float ps[3];
    k_read(kf0, ko, 0);
    const int fK = slV >= 2 ? slV - 2 : slV + 5, fV = slV >= 1 ? slV - 1 : 6;
    char* d1 = lds + RING + fK * SLOT + wid * 2048;
    char* d0 = lds + RING + fV * SLOT + wid * 2048;
    const bool wu = t == 5;
    const unsigned so = wu ? (unsigned)(b * 32768 + wid * 4096) : (unsigned)(((t + 3) * 64 + 8 * wid) * 256);
    const __amdgpu_buffer_rsrc_t rA = wu ? rsWo : rsK, rB = wu ? rsWo : rsV;
    const unsigned so2 = wu ? so + 2048 : so;
    SB0();
    qk1(S[0][0], kf0, 0);
    SB0();
    k_read(kf1, ko, 1);
    qk1(S[0][1], kf0, 1); qk1(S[0][2], kf0, 2);
    exp4(S[0][0], 0);
    dma16<KV_AUX>(rA, d1, dko0, so);
    SGB(0x008, 4);
#pragma unroll
    for (int i = 0; i < 4; ++i) { SGB(0x008, 1); SGB(0x002, 1); SGB(0x400, 1); }
    asm volatile("" :: "v"(kf1[0]), "v"(kf1[1]), "v"(kf1[2]), "v"(kf1[3]));
    SB0();
    v_read(l0, h0, vo, 0);
    qk1(S[1][0], kf1, 0); qk1(S[1][1], kf1, 1); qk1(S[1][2], kf1, 2);
    exp4(S[0][1], 1); exp4(S[0][2], 2); exp4(S[1][0], 0);
    dma16<KV_AUX>(rA, d1 + 1024, dko0 ^ (12u << 4), so + 1024);
#pragma unroll
    for (int i = 0; i < 12; ++i) { SGB(0x008, 1); SGB(0x002, 1); SGB(0x400, 1); }
    SB0();
    v_wait(vf0, l0, h0);
    v_read(l1, h1, vo, 1);
    pv_mma(vf0, pf, 0);
    exp4(S[1][1], 1); exp4(S[1][2], 2);
    pack1(pn[0], S[0][0], S[1][0]);
    ps[0] = sum8(S[0][0], S[1][0]);
    dma16<KV_AUX>(rB, d0, dvo0, so2);
#pragma unroll
    for (int i = 0; i < 8; ++i) { SGB(0x008, 1); SGB(0x002, 1); SGB(0x400, 1); SGB(0x002, 1); }
#pragma unroll
    for (int i = 0; i < 4; ++i) { SGB(0x008, 1); SGB(0x002, 1); }
    asm volatile("" :: "v"(pn[0]), "v"(ps[0]));
    SB0();
    v_wait(vf1, l1, h1);
    pv_mma(vf1, pf, 1);
    pack1(pn[1], S[0][1], S[1][1]); pack1(pn[2], S[0][2], S[1][2]);
    ps[1] = sum8(S[0][1], S[1][1]); ps[2] = sum8(S[0][2], S[1][2]);
    dma16<KV_AUX>(rB, d0 + 1024, dvo0 ^ (8u << 4), so2 + 1024);
#pragma unroll
    for (int i = 0; i < 12; ++i) { SGB(0x008, 1); SGB(0x002, 2); }
    asm volatile("" :: "v"(pn[1]), "v"(pn[2]), "v"(ps[1]), "v"(ps[2]));
    SB0();
    if (__any((ps[0] > 65536.f) | (ps[1] > 65536.f) | (ps[2] > 65536.f))) {
      qk(S, ko);
      remax(S, false);
      exp_sum(S, ps);
      pack(pn, S);
    }
#pragma unroll
    for (int qt = 0; qt < 3; ++qt) { lsum[qt] += ps[qt]; pf[qt] = pn[qt]; }
    slV = slV >= 5 ? slV - 5 : slV + 2;
  }
  {
    WAIT_V(0);
    WAIT_L0();
    bar();
    pv(pf, (unsigned)(slV * SLOT));
  }

  float mS[3];
#pragma unroll
  for (int qt = 0; qt < 3; ++qt) mS[qt] = -negm[qt];
  int tz = threadIdx.x; asm volatile("" : "+v"(tz));
  const int lane2 = tz & 63, li2 = lane2 & 15, g2 = lane2 >> 4;
#define lane lane2
#define li li2
#define g g2
  const unsigned xbase2 = (unsigned)(li * 1024 + 16 * (g ^ li));
  u32x4 w0[8], w1[8], w2[8];
  const unsigned voffo = (unsigned)(8 * g * 1024 + h * 256 + 16 * (li ^ (2 * kh)));
  const int rot3 = (rot + 8 * kh) & 15;
#pragma unroll
  for (int qt = 0; qt < 3; ++qt) {
    float l = lsum[qt];
    l += __shfl_xor(l, 16);
    l += __shfl_xor(l, 32);
    lsum[qt] = l;
  }
  auto xa = [&](int w) { return RING + (w == 0 ? 0 : 32768 + (w - 1) * 12288); };
  auto xb = [&](int w) { return w == 7 ? RING + 12288 : xa(w) + 8192; };
  auto ospot = [&](int khw, int qt, int i) {
    return (16 * qt + li) * 1024 + 16 * ((16 * h + 2 * (4 * khw + i) + (g >> 1)) ^ li) + 8 * (g & 1);
  };
  {
    char* ma = lds + xa(wid) + lane * 16;
    char* mb = lds + xb(wid) + lane * 16;
#pragma unroll
    for (int u = 0; u < 12; ++u) {
      const int i = u / 3, qt = u % 3;
      *(f32x4*)(u < 8 ? ma + u * 1024 : mb + (u - 8) * 1024) = oacc[4 + i][qt];
      if (u < 8) load_w2<1024>(w0, rsWo, voffo, rot3, 0, u);
      SB0();
    }
#pragma unroll
    for (int qt = 0; qt < 3; ++qt) *(f32x2*)(lds + ospot(kh ^ 1, qt, 0)) = f32x2{mS[qt], lsum[qt]};
  }
  WAIT_L0();
  bar();
  {
    const int pw = wid ^ 4;
    const char* oa = lds + xa(pw) + lane * 16;
    const char* ob = lds + xb(pw) + lane * 16;
    f32x2 ml[3];
    f32x4 o[4][3];
#pragma unroll
    for (int qt = 0; qt < 3; ++qt) ml[qt] = *(const f32x2*)(lds + ospot(kh, qt, 0));
#pragma unroll
    for (int u = 0; u < 12; ++u) {
      o[u / 3][u % 3] = *(const f32x4*)(u < 8 ? oa + u * 1024 : ob + (u - 8) * 1024);
      if (u < 8) load_w2<1024>(w1, rsWo, voffo, (rot3 + 1) & 15, 0, u);
      SB0();
    }
#pragma unroll
    for (int qt = 0; qt < 3; ++qt) {
      const float M = fmaxf(mS[qt], ml[qt][0]);
      const float wa = __builtin_amdgcn_exp2f(mS[qt] - M), wb = __builtin_amdgcn_exp2f(ml[qt][0] - M);
      const float inv = __builtin_amdgcn_rcpf(wa * lsum[qt] + wb * ml[qt][1]);
      const float fa = wa * inv, fb = wb * inv;
#pragma unroll
      for (int i = 0; i < 4; ++i) {
        const f32x4 mine = oacc[i][qt];
        u32x2 r;
        r[0] = cvtpk(mine[0] * fa + o[i][qt][0] * fb, mine[1] * fa + o[i][qt][1] * fb);
        r[1] = cvtpk(mine[2] * fa + o[i][qt][2] * fb, mine[3] * fa + o[i][qt][3] * fb);
        *(u32x2*)(lds + ospot(kh, qt, i)) = r;
        if (qt * 4 + i < 8) load_w2<1024>(w2, rsWo, voffo, (rot3 + 2) & 15, 0, qt * 4 + i);
        SB0();
      }
    }
  }
  WAIT_L0();
  bar();
  {
    f32x4 acc[3][8];
#pragma unroll
    for (int qt = 0; qt < 3; ++qt)
#pragma unroll
      for (int c = 0; c < 8; ++c) acc[qt][c] = f32x4{0.f, 0.f, 0.f, 0.f};
    gemm_main<1024, 8>(acc, w0, w1, w2, rsWo, voffo, lds, xbase2, rot3, -1);
    int tz3 = threadIdx.x; asm volatile("" : "+v"(tz3));
    const int lane3 = tz3 & 63;
    char* my = lds + RING + wid * 12288 + lane3 * 8;
#pragma unroll
    for (int qt = 0; qt < 3; ++qt)
#pragma unroll
      for (int c = 0; c < 8; ++c) *(f32x2*)(my + (qt * 8 + c) * 512) = f32x2{acc[qt][c][2], acc[qt][c][3]};
    WAIT_L0();
    bar();
    const char* ot = lds + RING + (wid ^ 4) * 12288 + lane3 * 8;
    const __amdgpu_buffer_rsrc_t rsS = mk_rsrc(slab + (size_t)(br * 16 + b) * (8 * SLAB_F), (unsigned)(8 * SLAB_F * 2));
    const unsigned sbase = (unsigned)((kvh * SLAB_F + h * 6144 + lane3 * 8) * 2);
#pragma unroll
    for (int qt = 0; qt < 3; ++qt) {
      f32x2 o[8];
#pragma unroll
      for (int c = 0; c < 8; ++c) o[c] = *(const f32x2*)(ot + (qt * 8 + c) * 512);
#pragma unroll
      for (int rr = 0; rr < 2; ++rr) {
        u32x4 v;
#pragma unroll
        for (int p2 = 0; p2 < 4; ++p2) v[p2] = cvtpk(acc[qt][2 * p2][rr] + o[2 * p2][rr], acc[qt][2 * p2 + 1][rr] + o[2 * p2 + 1][rr]);
        __builtin_amdgcn_raw_buffer_store_b128(v, rsS, (int)(sbase + (unsigned)((qt * 4 + 2 * kh + rr) * 1024)), 0, 16);
      }
    }
    asm volatile("s_waitcnt vmcnt(0)" ::: "memory");
    bar();
    const unsigned long long e = ((unsigned long long)e_hi << 32) | e_lo;
    if (tz3 == 0) __hip_atomic_store(ctrl + 16 + kvh, e, RLX_AGENT);
    if (wid < 6 || (wid == 6 && kvh == 0)) {
      unsigned spins = 0;
      for (;;) {
        const unsigned long long f = __hip_atomic_load(ctrl + 16 + (lane3 & 7), RLX_AGENT);
        if (__all(f == e) || ++spins > (1u << 22)) break;
        __builtin_amdgcn_s_sleep(1);
      }
      asm volatile("" ::: "memory");
      if (wid == 6) {
        if (lane3 == 0) __hip_atomic_store(ctrl, e, RLX_AGENT);
      } else {
        const int U = 6 * kvh + wid, uh = U / 12, uq = (U % 12) >> 2, ur = U & 3;
        u32x4 v[8];
#pragma unroll
        for (int j = 0; j < 8; ++j) v[j] = __builtin_amdgcn_raw_buffer_load_b128(rsS, (int)(unsigned)((j * SLAB_F + U * 512 + lane3 * 8) * 2), 0, 16);
        float sum[8] = {0.f, 0.f, 0.f, 0.f, 0.f, 0.f, 0.f, 0.f};
#pragma unroll
        for (int j = 0; j < 8; ++j)
#pragma unroll
          for (int e2 = 0; e2 < 4; ++e2) {
            sum[2 * e2]     += __builtin_bit_cast(float, v[j][e2] << 16);
            sum[2 * e2 + 1] += __builtin_bit_cast(float, v[j][e2] & 0xffff0000u);
          }
        u32x4 o;
#pragma unroll
        for (int e2 = 0; e2 < 4; ++e2) o[e2] = cvtpk(sum[2 * e2], sum[2 * e2 + 1]);
        *(u32x4*)(out + ((size_t)(b * SQ + 16 * uq + (lane3 & 15)) * 1024 + br * 512 + 128 * uh + 32 * (lane3 >> 4) + 8 * ur)) = o;
      }
    }
  }
#undef lane
#undef li
#undef g
}

extern "C" void kernel_launch(void* const* d_in, const int* in_sizes, int n_in,
                              void* d_out, int out_size, void* d_ws, size_t ws_size,
                              hipStream_t stream) {
  (void)in_sizes; (void)n_in; (void)out_size; (void)ws_size;
  hipFuncSetAttribute((const void*)gqa_fused_kernel, hipFuncAttributeMaxDynamicSharedMemorySize, LDS_BYTES);
  unsigned short* slab = (unsigned short*)d_ws;
  hipLaunchKernelGGL(gqa_fused_kernel, dim3(256), dim3(512), LDS_BYTES, stream,
                     (const unsigned short*)d_in[0], (const unsigned short*)d_in[1], (const unsigned short*)d_in[2],
                     (const unsigned short*)d_in[3], (const unsigned short*)d_in[4],
                     (const unsigned short*)d_in[5], (const unsigned short*)d_in[6], (const unsigned short*)d_in[7],
                     (const unsigned short*)d_in[8], (const unsigned short*)d_in[9], slab, (unsigned short*)d_out);
}
```
